```python
import torch

def _softmax(x, axis=-1):
    return torch.softmax(x, dim=axis)
def _gelu(x):
    return torch.nn.functional.gelu(x, approximate="tanh")
def _silu(x):
    return torch.nn.functional.silu(x)
def _sigmoid(x):
    return torch.sigmoid(x)
def _layernorm(x, axis=-1, eps=1e-5):
    m = x.mean(dim=axis, keepdim=True)
    v = x.var(dim=axis, keepdim=True, unbiased=False)
    return (x - m) / torch.sqrt(v + eps)
def _scatter_add(tgt, idx, src, axis=0):
    return tgt.clone().index_add_(axis, idx, src)
def _segment_sum(src, seg, n):
    out = torch.zeros((n,) + src.shape[1:], dtype=src.dtype)
    return out.index_add_(0, seg, src)
def _pad(x, pw):
    flat = tuple(v for pair in reversed(pw) for v in pair)
    return torch.nn.functional.pad(x, flat)

kernel_name = 'recurrent_scan__spectral_gfnet__swiglu_ffn__prompt'

def setup_inputs():

    _w = (lambda a: (lambda t: t.to(torch.bfloat16) if t.is_floating_point() else t)(torch.as_tensor(a)))
    def _A_setup():
        B, S, D = 1, 2048, 448
        Df = D // 2 + 1
        return {
            "x": _w((lambda *s: torch.randn(*s, dtype=torch.bfloat16) * 0.05)(B, S, D)),
            "w_r": _w((lambda *s: torch.randn(*s, dtype=torch.bfloat16) * 0.2)(S, Df) * 0.02),
            "w_i": _w((lambda *s: torch.randn(*s, dtype=torch.bfloat16) * 0.2)(S, Df) * 0.02),
        }
    _A_in = _A_setup()

    def _B_setup():
        B, S, D, F = 1, 2048, 448, 1792
        return {
            "x": _w((lambda *s: torch.randn(*s, dtype=torch.bfloat16) * 0.05)(B, S, D)),
            "w_gate": _w((lambda *s: torch.randn(*s, dtype=torch.bfloat16) * 0.2)(D, F)),
            "w_up": _w((lambda *s: torch.randn(*s, dtype=torch.bfloat16) * 0.2)(D, F)),
            "w_down": _w((lambda *s: torch.randn(*s, dtype=torch.bfloat16) * 0.2)(F, D)),
        }
    _B_in = _B_setup()

    return {
        "A_x": _A_in["x"],
        "A_w_r": _A_in["w_r"],
        "A_w_i": _A_in["w_i"],
        "B_x": _B_in["x"],
        "B_w_gate": _B_in["w_gate"],
        "B_w_up": _B_in["w_up"],
        "B_w_down": _B_in["w_down"],
    }

def reference(A_x, A_w_r, A_w_i, B_x, B_w_gate, B_w_up, B_w_down):
    def _A_ref(x, w_r, w_i):
        B, S, D = x.shape
        X = (lambda x, axes=(-2,-1): torch.fft.rfft2(x.float(), dim=axes))(x, axes=(-2, -1))
        W = w_r + 1j * w_i
        X = X * W
        y = (lambda x, s, axes=(-2,-1): torch.fft.irfft2(x, s=s, dim=axes).to(torch.bfloat16))(X, (S, D), axes=(-2, -1))
        return y

    def _B_ref(x, w_gate, w_up, w_down):
        g = _silu(x @ w_gate)
        u = x @ w_up
        return (g * u) @ w_down

    x = A_x
    xn = _layernorm(x, axis=-1)
    s = _A_ref(xn, A_w_r, A_w_i)
    g = _silu(_B_ref(xn, B_w_gate, B_w_up, B_w_down))
    return x + s * g
```

```cpp
#include <hip/hip_runtime.h>
#include <hip/hip_bf16.h>

#if defined(__HIP_DEVICE_COMPILE__)
namespace tb {
typedef unsigned short u16;
constexpr double PI = 3.14159265358979323846264338327950288;
constexpr double tsin(double x) { double t = x, s = x, x2 = x * x; for (int i = 1; i < 14; ++i) { t *= -x2 / ((2 * i) * (2 * i + 1)); s += t; } return s; }
constexpr double tcos(double x) { double t = 1, s = 1, x2 = x * x; for (int i = 1; i < 14; ++i) { t *= -x2 / ((2 * i - 1) * (2 * i)); s += t; } return s; }
constexpr double cos2pi(int n, int N) {
  n %= N;
  if (2 * n > N) n = N - n;
  double sg = 1.0;
  if (4 * n > N) { n = N / 2 - n; sg = -1.0; }
  if (4 * n == N) return 0.0;
  return sg * ((8 * n > N) ? tsin(2 * PI * (double)(N / 4 - n) / N) : tcos(2 * PI * (double)n / N));
}
constexpr u16 f2bf(double v) { float f = (float)v; unsigned u = __builtin_bit_cast(unsigned, f); u += 0x7FFFu + ((u >> 16) & 1u); return (u16)(u >> 16); }
struct Base { u16 c[448]; u16 c2[448]; u16 c1[448]; u16 e[64]; float cf[448]; };
constexpr Base mkbase() {
  Base b{};
  for (int i = 0; i < 448; ++i) { double c = cos2pi(i, 448); b.c[i] = f2bf(c); b.cf[i] = __builtin_bit_cast(float, (unsigned)b.c[i] << 16); b.c2[i] = f2bf(c * 2.0 / 448.0 / 2048.0); b.c1[i] = f2bf(c / 448.0 / 2048.0); }
  for (int i = 0; i < 64; ++i) b.e[i] = f2bf(cos2pi(i, 64));
  return b;
}
constexpr Base B = mkbase();
struct TFc { u16 v[28 * 512]; };
constexpr TFc mktf(int c) {
  TFc t{};
  for (int r = 0; r < 32; ++r) {
    int slot = 32 * c + r, k = slot == 1 ? 224 : slot >> 1, ph = ((slot & 1) && slot != 1) ? 112 : 0;
    for (int d = 0; d < 448; ++d) t.v[(d >> 4) * 512 + (((d >> 2) & 1) * 32 + r) * 8 + ((d >> 3) & 1) * 4 + (d & 3)] = B.c[(k * d + ph) % 448];
  }
  return t;
}
struct TIc { u16 v[2 * 14 * 512]; };
constexpr TIc mkti(int c3) {
  TIc t{};
  for (int par = 0; par < 2; ++par)
    for (int i = 0; i < 224; ++i) {
      int slot = 4 * (i >> 1) + 2 * par + (i & 1), k = slot == 1 ? 224 : slot >> 1, ph = ((slot & 1) && slot != 1) ? 112 : 0;
      const u16* src = slot < 2 ? B.c1 : B.c2;
      int o = (par * 14 + (i >> 4)) * 512 + ((i >> 3) & 1) * 256 + (i & 7);
      for (int r = 0; r < 32; ++r) t.v[o + r * 8] = src[(k * (32 * c3 + 16 * ((r >> 2) & 1) + 4 * (r >> 3) + (r & 3)) + ph) % 448];
    }
  return t;
}
struct FCc { u16 v[20 * 512]; };
constexpr FCc mkfc() {
  FCc t{};
  for (int tile = 0; tile < 4; ++tile) for (int half = 0; half < 2; ++half) for (int cs = 0; cs < 2; ++cs)
    for (int l = 0; l < 64; ++l) for (int e = 0; e < 8; ++e) {
      int g = l >> 4, s1 = 32 * half + 16 * (g & 1) + 8 * (e >> 2) + 4 * (g >> 1) + (e & 3);
      t.v[((tile * 2 + half) * 2 + cs) * 512 + l * 8 + e] = B.e[((16 * tile + (l & 15)) * s1 + 48 * cs) % 64];
    }
  for (int T = 0; T < 2; ++T) for (int cs = 0; cs < 2; ++cs)
    for (int l = 0; l < 64; ++l) for (int e = 0; e < 8; ++e) {
      int g = l >> 4, s2 = 16 * (e >> 2) + 4 * g + (e & 3);
      t.v[(16 + T * 2 + cs) * 512 + l * 8 + e] = B.e[(2 * (16 * T + (l & 15)) * s2 + 48 * cs) % 64];
    }
  return t;
}
struct CSc { float v[512]; };
constexpr CSc mkcs() {
  CSc t{};
  for (int slot = 0; slot < 448; ++slot) {
    int k = slot == 1 ? 224 : slot >> 1, ph = ((slot & 1) && slot != 1) ? 112 : 0;
    double a = 0;
    for (int d = 0; d < 448; ++d) a += B.cf[(k * d + ph) % 448];
    t.v[slot] = (float)a;
  }
  return t;
}
constexpr CSc cs = mkcs();
#define TFV(i) constexpr TFc tf##i = mktf(i);
TFV(0) TFV(1) TFV(2) TFV(3) TFV(4) TFV(5) TFV(6) TFV(7) TFV(8) TFV(9) TFV(10) TFV(11) TFV(12) TFV(13)
#define TIV(i) constexpr TIc ti##i = mkti(i);
TIV(0) TIV(1) TIV(2) TIV(3) TIV(4) TIV(5) TIV(6)
constexpr FCc fc = mkfc();
struct All { TFc tf[14]; TIc ti[7]; FCc fc; CSc cs; };
}
__device__ __attribute__((aligned(1024))) const tb::All g_tab = {
  {tb::tf0, tb::tf1, tb::tf2, tb::tf3, tb::tf4, tb::tf5, tb::tf6, tb::tf7, tb::tf8, tb::tf9, tb::tf10, tb::tf11, tb::tf12, tb::tf13},
  {tb::ti0, tb::ti1, tb::ti2, tb::ti3, tb::ti4, tb::ti5, tb::ti6}, tb::fc, tb::cs};
#endif

__global__ void __launch_bounds__(256, 1)
gfnet_swiglu_kernel(const void* x, const void* wr, const void* wi, const void* wg, const void* wu, const void* wd, void* out, void* ws) {
#if defined(__HIP_DEVICE_COMPILE__)
  __shared__ __attribute__((aligned(1024))) char lds[163840];
  unsigned tid = threadIdx.x;
  const void* tab = (const void*)&g_tab;
  asm volatile(
    "s_load_dwordx16 s[4:19], s[0:1], 0x0\n"
    "v_lshrrev_b32 v1, 6, v0\n"
    "s_nop 0\n"
    "v_readfirstlane_b32 s44, v1\n"
    "v_and_b32 v0, 63, v0\n"
    "v_lshlrev_b32 v1, 4, v0\n"
    "s_cmp_lg_u32 s44, 3\n"
    "s_cbranch_scc1 PF1_%=\n"
    "s_getpc_b64 s[72:73]\n"
    "PFBASE_%=:\n"
    "s_lshr_b32 s51, s2, 3\n"
    "s_lshl_b32 s51, s51, 10\n"
    "s_min_u32 s51, s51, KEND_%=-PFBASE_%=-1024\n"
    "s_add_u32 s72, s72, s51\n"
    "s_addc_u32 s73, s73, 0\n"
    "global_load_dwordx4 v[200:203], v1, s[72:73]\n"
    "PF1_%=:\n"
    "s_cmp_ge_u32 s2, 224\n"
    "s_cbranch_scc1 AUX2_%=\n"
    "; ==== main ====\n"
    "s_and_b32 s51, s2, 7\n"
    "s_lshr_b32 s52, s2, 3\n"
    "s_mul_i32 s55, s52, 37\n"
    "s_lshr_b32 s55, s55, 8\n"
    "s_mul_i32 s56, s55, 7\n"
    "s_sub_u32 s56, s52, s56\n"
    "s_lshr_b32 s52, s51, 1\n"
    "s_lshl2_add_u32 s53, s52, s55\n"
    "s_lshl2_add_u32 s55, s51, s55\n"
    "s_and_b32 s51, s51, 1\n"
    "s_mul_i32 s51, s51, 7\n"
    "s_add_u32 s54, s51, s56\n"
    "s_mov_b32 s36, s20\n"
    "s_and_b32 s37, s21, 0xffff\n"
    "s_mov_b32 s38, 624640\n"
    "s_mov_b32 s39, 0x20000\n"
    "s_mul_i32 s76, s54, 28672\n"
    "s_lshl_b32 s70, s44, 10\n"
    "s_add_u32 s76, s76, s70\n"
    "s_lshr_b32 s51, s53, 2\n"
    "s_lshl1_add_u32 s51, s51, s56\n"
    "s_mul_i32 s51, s51, 128\n"
    "s_cmp_ge_u32 s51, 896\n"
    "s_cselect_b32 s52, 896, 0\n"
    "s_sub_u32 s80, s51, s52\n"
    "s_add_u32 s81, s80, 128\n"
    "s_cmp_eq_u32 s81, 896\n"
    "s_cselect_b32 s81, 0, s81\n"
    "s_add_u32 s82, s81, 128\n"
    "s_cmp_eq_u32 s82, 896\n"
    "s_cselect_b32 s82, 0, s82\n"
    "s_add_u32 s83, s82, 128\n"
    "s_cmp_eq_u32 s83, 896\n"
    "s_cselect_b32 s83, 0, s83\n"
    "s_add_u32 s84, s83, 128\n"
    "s_cmp_eq_u32 s84, 896\n"
    "s_cselect_b32 s84, 0, s84\n"
    "s_add_u32 s85, s84, 128\n"
    "s_cmp_eq_u32 s85, 896\n"
    "s_cselect_b32 s85, 0, s85\n"
    "s_add_u32 s86, s85, 128\n"
    "s_cmp_eq_u32 s86, 896\n"
    "s_cselect_b32 s86, 0, s86\n"
    "s_lshl_b32 s59, s80, 5\n"
    "s_add_u32 m0, s70, 114688\n"
    "s_add_u32 s51, s76, s59\n"
    "buffer_load_dwordx4 v1, s[36:39], s51 offen lds\n"
    "s_lshl_b32 s59, s81, 5\n"
    "s_add_u32 m0, s70, 118784\n"
    "s_add_u32 s51, s76, s59\n"
    "buffer_load_dwordx4 v1, s[36:39], s51 offen lds\n"
    "s_lshl_b32 s59, s82, 5\n"
    "s_add_u32 m0, s70, 122880\n"
    "s_add_u32 s51, s76, s59\n"
    "buffer_load_dwordx4 v1, s[36:39], s51 offen lds\n"
    "s_lshr_b32 s52, s44, 1\n"
    "s_lshl1_add_u32 s57, s53, s52\n"
    "s_and_b32 s58, s44, 1\n"
    "s_lshl_b32 s46, s58, 10\n"
    "s_add_u32 s46, s46, s57\n"
    "s_mul_i32 s46, s46, 896\n"
    "v_lshrrev_b32 v176, 3, v0\n"
    "v_mul_u32_u24 v2, 0x7000, v176\n"
    "v_and_b32 v177, 7, v0\n"
    "v_lshrrev_b32 v3, 4, v0\n"
    "v_xor_b32 v3, v3, v177\n"
    "v_lshl_add_u32 v2, v3, 4, v2\n"
    "v_xor_b32 v3, 64, v2\n"
    "s_mul_i32 s45, s44, 28672\n"
    "s_add_u32 s47, s46, 229376\n"
    "s_add_u32 s48, s46, 458752\n"
    "s_add_u32 s49, s46, 688128\n"
    "s_waitcnt lgkmcnt(0)\n"
    "s_mov_b32 s24, s4\n"
    "s_and_b32 s25, s5, 0xffff\n"
    "s_mov_b32 s26, 1835008\n"
    "s_mov_b32 s27, 0x20000\n"
    "s_add_u32 m0, s45, 0\n"
    "s_add_u32 s51, s46, s80\n"
    "buffer_load_dwordx4 v2, s[24:27], s51 offen lds\n"
    "s_add_u32 m0, s45, 1024\n"
    "s_add_u32 s51, s47, s80\n"
    "buffer_load_dwordx4 v3, s[24:27], s51 offen lds\n"
    "s_add_u32 m0, s45, 2048\n"
    "s_add_u32 s51, s48, s80\n"
    "buffer_load_dwordx4 v2, s[24:27], s51 offen lds\n"
    "s_add_u32 m0, s45, 3072\n"
    "s_add_u32 s51, s49, s80\n"
    "buffer_load_dwordx4 v3, s[24:27], s51 offen lds\n"
    "s_load_dwordx2 s[62:63], s[18:19], 0x0\n"
    "v_and_b32 v178, 31, v0\n"
    "v_bfe_u32 v6, v0, 1, 3\n"
    "v_lshrrev_b32 v5, 5, v0\n"
    "v_xor_b32 v6, v6, v5\n"
    "v_lshlrev_b32 v6, 4, v6\n"
    "v_lshl_add_u32 v5, v178, 7, v6\n"
    "v_add_u32 v5, s45, v5\n"
    "v_xor_b32 v6, 32, v5\n"
    "v_xor_b32 v7, 64, v5\n"
    "v_xor_b32 v8, 96, v5\n"
    "s_cmp_lt_u32 s44, 2\n"
    "s_cselect_b64 s[72:73], s[10:11], s[12:13]\n"
    "s_mov_b32 s28, s72\n"
    "s_and_b32 s29, s73, 0xffff\n"
    "s_mov_b32 s30, 1605632\n"
    "s_mov_b32 s31, 0x20000\n"
    "s_mov_b32 s32, s18\n"
    "s_and_b32 s33, s19, 0xffff\n"
    "s_mov_b32 s34, 15433728\n"
    "s_add_u32 m0, s45, 4096\n"
    "s_add_u32 s51, s46, s81\n"
    "buffer_load_dwordx4 v2, s[24:27], s51 offen lds\n"
    "s_mov_b32 s35, 0x20000\n"
    "s_mov_b32 s40, s16\n"
    "s_and_b32 s41, s17, 0xffff\n"
    "s_mov_b32 s42, 1835008\n"
    "s_mov_b32 s43, 0x20000\n"
    "s_mul_i32 s50, s58, 114688\n"
    "s_lshl_b32 s22, s54, 8\n"
    "s_add_u32 s50, s50, s22\n"
    "s_lshl_b32 s71, s44, 12\n"
    "s_mul_i32 s4, s80, 1792\n"
    "s_add_u32 s4, s4, s50\n"
    "s_mul_i32 s5, s81, 1792\n"
    "s_add_u32 s5, s5, s50\n"
    "s_mul_i32 s6, s82, 1792\n"
    "s_add_u32 s6, s6, s50\n"
    "s_mul_i32 s7, s83, 1792\n"
    "s_add_u32 s7, s7, s50\n"
    "s_add_u32 m0, s45, 5120\n"
    "s_add_u32 s51, s47, s81\n"
    "buffer_load_dwordx4 v3, s[24:27], s51 offen lds\n"
    "s_mul_i32 s8, s84, 1792\n"
    "s_add_u32 s8, s8, s50\n"
    "s_mul_i32 s9, s85, 1792\n"
    "s_add_u32 s9, s9, s50\n"
    "s_mul_i32 s10, s86, 1792\n"
    "s_add_u32 s10, s10, s50\n"
    "v_mul_u32_u24 v4, 3584, v176\n"
    "v_and_b32 v176, 2, v176\n"
    "s_lshr_b32 s22, s44, 1\n"
    "s_lshl_b32 s22, s22, 2\n"
    "v_or_b32 v176, s22, v176\n"
    "v_xor_b32 v176, v176, v177\n"
    "v_lshl_add_u32 v4, v176, 4, v4\n"
    "v_add_u32 v252, 27648, v4\n"
    "v_add_u32 v253, 55296, v4\n"
    "v_add_u32 v254, 82944, v4\n"
    "s_add_u32 m0, s45, 6144\n"
    "s_add_u32 s51, s48, s81\n"
    "buffer_load_dwordx4 v2, s[24:27], s51 offen lds\n"
    "v_bfe_u32 v176, v0, 2, 2\n"
    "v_lshrrev_b32 v177, 5, v0\n"
    "v_lshl_add_u32 v177, v177, 2, v176\n"
    "v_lshlrev_b32 v9, 7, v177\n"
    "v_bfe_u32 v176, v0, 3, 2\n"
    "v_lshl_add_u32 v9, v176, 5, v9\n"
    "v_and_b32 v176, 3, v0\n"
    "v_lshl_add_u32 v9, v176, 3, v9\n"
    "v_bfe_u32 v178, v0, 4, 1\n"
    "v_lshlrev_b32 v220, 13, v178\n"
    "v_add_u32 v220, v220, v9\n"
    "v_add_u32 v14, 114688, v220\n"
    "v_mul_u32_u24 v178, 57344, v178\n"
    "v_add_u32 v9, v9, v178\n"
    "v_xor_b32 v10, 32, v9\n"
    "s_add_u32 m0, s45, 7168\n"
    "s_add_u32 s51, s49, s81\n"
    "buffer_load_dwordx4 v3, s[24:27], s51 offen lds\n"
    "v_xor_b32 v11, 64, v9\n"
    "v_xor_b32 v12, 96, v9\n"
    "v_add_u32 v13, 114688, v1\n"
    "v_and_b32 v176, 15, v0\n"
    "v_lshrrev_b32 v177, 4, v0\n"
    "v_lshlrev_b32 v177, 4, v177\n"
    "v_lshl_add_u32 v220, v176, 12, v177\n"
    "s_mov_b32 s64, 0x3F803F80\n"
    "s_mov_b32 s77, 0xFFFF0000\n"
    "s_lshl_b32 s65, s54, 17\n"
    "s_lshl_b32 s22, s57, 7\n"
    "s_add_u32 s65, s65, s22\n"
    "s_lshl_b32 s22, s58, 6\n"
    "s_add_u32 s65, s65, s22\n"
    "s_add_u32 s65, s65, 262144\n"
    "v_bfe_u32 v176, v0, 1, 4\n"
    "s_add_u32 m0, s45, 8192\n"
    "s_add_u32 s51, s46, s82\n"
    "buffer_load_dwordx4 v2, s[24:27], s51 offen lds\n"
    "v_lshrrev_b32 v177, 5, v0\n"
    "v_lshlrev_b32 v249, 4, v177\n"
    "v_lshlrev_b32 v177, 2, v177\n"
    "v_sub_u32 v176, v176, v177\n"
    "v_and_b32 v177, 1, v0\n"
    "v_lshlrev_b32 v177, 4, v177\n"
    "v_mov_b32 v178, 0x3f80\n"
    "v_lshlrev_b32 v177, v177, v178\n"
    "v_cmp_eq_u32 vcc, 0, v176\n"
    "s_nop 1\n"
    "v_cndmask_b32 v178, 0, v177, vcc\n"
    "v_accvgpr_write_b32 a0, v178\n"
    "v_cmp_eq_u32 vcc, 1, v176\n"
    "s_nop 1\n"
    "v_cndmask_b32 v178, 0, v177, vcc\n"
    "v_accvgpr_write_b32 a1, v178\n"
    "v_cmp_eq_u32 vcc, 2, v176\n"
    "s_add_u32 m0, s45, 9216\n"
    "s_add_u32 s51, s47, s82\n"
    "buffer_load_dwordx4 v3, s[24:27], s51 offen lds\n"
    "v_cndmask_b32 v178, 0, v177, vcc\n"
    "v_accvgpr_write_b32 a2, v178\n"
    "v_cmp_eq_u32 vcc, 3, v176\n"
    "s_nop 1\n"
    "v_cndmask_b32 v178, 0, v177, vcc\n"
    "v_accvgpr_write_b32 a3, v178\n"
    "v_cmp_eq_u32 vcc, 8, v176\n"
    "s_nop 1\n"
    "v_cndmask_b32 v178, 0, v177, vcc\n"
    "v_accvgpr_write_b32 a4, v178\n"
    "v_cmp_eq_u32 vcc, 9, v176\n"
    "s_nop 1\n"
    "v_cndmask_b32 v178, 0, v177, vcc\n"
    "v_accvgpr_write_b32 a5, v178\n"
    "v_cmp_eq_u32 vcc, 10, v176\n"
    "s_nop 1\n"
    "v_cndmask_b32 v178, 0, v177, vcc\n"
    "v_accvgpr_write_b32 a6, v178\n"
    "v_cmp_eq_u32 vcc, 11, v176\n"
    "s_add_u32 m0, s45, 10240\n"
    "s_add_u32 s51, s48, s82\n"
    "buffer_load_dwordx4 v2, s[24:27], s51 offen lds\n"
    "v_cndmask_b32 v178, 0, v177, vcc\n"
    "v_accvgpr_write_b32 a7, v178\n"
    "s_lshl_b32 s22, s44, 8\n"
    "s_add_u32 s22, s22, 143360\n"
    "v_add_u32 v249, s22, v249\n"
    "v_lshl_add_u32 v248, v0, 2, s22\n"
    "s_mul_i32 s66, s57, 229376\n"
    "s_lshl1_add_u32 s22, s54, s58\n"
    "s_lshl_b32 s22, s22, 10\n"
    "s_add_u32 s66, s66, s22\n"
    "s_add_u32 s66, s66, 3932160\n"
    "s_lshl2_add_u32 s22, s56, s44\n"
    "s_mul_i32 s67, s22, 57344\n"
    "s_add_u32 s67, s67, 11272192\n"
    "s_lshl2_add_u32 s22, s55, s44\n"
    "s_mul_i32 s68, s22, 57344\n"
    "s_add_u32 s68, s68, 3932160\n"
    "s_add_u32 m0, s45, 11264\n"
    "s_add_u32 s51, s49, s82\n"
    "buffer_load_dwordx4 v3, s[24:27], s51 offen lds\n"
    "s_mov_b32 s69, s2\n"
    "s_lshl_b32 s74, s69, 1\n"
    "s_cmp_eq_u32 s69, 224\n"
    "s_cselect_b32 s74, 1, s74\n"
    "s_cmp_lt_u32 s74, 2\n"
    "s_cselect_b32 s75, 1879048192, 0\n"
    "v_lshrrev_b32 v250, 3, v0\n"
    "v_and_b32 v251, 7, v0\n"
    "v_lshrrev_b32 v176, 4, v0\n"
    "v_xor_b32 v251, v251, v176\n"
    "v_lshlrev_b32 v251, 4, v251\n"
    "v_lshl_add_u32 v250, v250, 7, v251\n"
    "v_xor_b32 v251, 64, v250\n"
    "s_lshr_b32 s52, s44, 1\n"
    "s_add_u32 s98, s74, s52\n"
    "s_lshl_b32 s98, s98, 12\n"
    "s_add_u32 m0, s45, 12288\n"
    "s_add_u32 s51, s46, s83\n"
    "buffer_load_dwordx4 v2, s[24:27], s51 offen lds\n"
    "s_mul_i32 s52, s52, s75\n"
    "s_add_u32 s98, s98, s52\n"
    "s_and_b32 s52, s44, 1\n"
    "s_lshl_b32 s52, s52, 11\n"
    "s_add_u32 s98, s98, s52\n"
    "s_add_u32 s98, s98, 262144\n"
    "s_lshl2_add_u32 s99, s69, s44\n"
    "s_lshl_b32 s99, s99, 11\n"
    "s_add_u32 s99, s99, 12877824\n"
    "s_mul_i32 s79, s44, 458752\n"
    "s_lshl_b32 s52, s74, 7\n"
    "s_add_u32 s79, s79, s52\n"
    "s_add_u32 s79, s79, 2097152\n"
    "s_lshl4_add_u32 s78, s54, s53\n"
    "s_lshl_b32 s78, s78, 7\n"
    "s_add_u32 s78, s78, 32768\n"
    "s_lshl4_add_u32 s3, s53, s54\n"
    "s_add_u32 m0, s45, 13312\n"
    "s_add_u32 s51, s47, s83\n"
    "buffer_load_dwordx4 v3, s[24:27], s51 offen lds\n"
    "s_lshl_b32 s3, s3, 7\n"
    "s_add_u32 s3, s3, 65536\n"
    "s_lshr_b32 s22, s74, 5\n"
    "s_lshl_b32 s22, s22, 11\n"
    "s_add_u32 s22, s22, 32768\n"
    "v_min_u32 v176, 47, v0\n"
    "v_lshlrev_b32 v176, 7, v176\n"
    "v_add_u32 v177, s22, v176\n"
    "v_add_u32 v176, 6144, v176\n"
    "v_cmp_gt_u32 vcc, 16, v0\n"
    "s_nop 1\n"
    "v_cndmask_b32 v217, v176, v177, vcc\n"
    "v_min_u32 v216, 27, v0\n"
    "v_lshlrev_b32 v216, 7, v216\n"
    "v_add_u32 v216, 4096, v216\n"
    "s_lshr_b32 s22, s55, 1\n"
    "s_lshl_b32 s22, s22, 11\n"
    "s_add_u32 m0, s45, 14336\n"
    "s_add_u32 s51, s48, s83\n"
    "buffer_load_dwordx4 v2, s[24:27], s51 offen lds\n"
    "s_add_u32 s22, s22, 65536\n"
    "v_min_u32 v221, 13, v0\n"
    "v_lshl_add_u32 v221, v221, 7, s22\n"
    "s_lshl_b32 s22, s69, 3\n"
    "s_add_u32 s22, s22, 131072\n"
    "s_waitcnt lgkmcnt(0)\n"
    "s_mul_i32 s60, s62, 0x9E3779B1\n"
    "s_add_u32 s60, s60, 0x7F4A7C15\n"
    "s_mul_i32 s61, s63, 0x85EBCA77\n"
    "s_add_u32 s61, s61, 0x165667B1\n"
    "v_mov_b32 v222, s60\n"
    "v_mov_b32 v223, s61\n"
    "v_mov_b32 v168, 0\n"
    "v_mov_b32 v169, 0\n"
    "; ---- x lands: fragments, statistics, W(pass 0) into the freed slots ----\n"
    "s_waitcnt vmcnt(11)\n"
    "s_add_u32 m0, s45, 15360\n"
    "s_add_u32 s51, s49, s83\n"
    "buffer_load_dwordx4 v3, s[24:27], s51 offen lds\n"
    "ds_read_b128 v[16:19], v5 offset:0\n"
    "ds_read_b128 v[20:23], v6 offset:0\n"
    "ds_read_b128 v[24:27], v7 offset:0\n"
    "ds_read_b128 v[28:31], v8 offset:0\n"
    "s_waitcnt vmcnt(8)\n"
    "ds_read_b128 v[32:35], v5 offset:4096\n"
    "ds_read_b128 v[36:39], v6 offset:4096\n"
    "ds_read_b128 v[40:43], v7 offset:4096\n"
    "ds_read_b128 v[44:47], v8 offset:4096\n"
    "s_waitcnt lgkmcnt(4)\n"
    "v_dot2c_f32_bf16 v168, s64, v16\n"
    "v_dot2c_f32_bf16 v169, v16, v16\n"
    "s_add_u32 m0, s45, 16384\n"
    "s_add_u32 s51, s46, s84\n"
    "buffer_load_dwordx4 v2, s[24:27], s51 offen lds\n"
    "v_dot2c_f32_bf16 v168, s64, v17\n"
    "v_dot2c_f32_bf16 v169, v17, v17\n"
    "v_dot2c_f32_bf16 v168, s64, v18\n"
    "v_dot2c_f32_bf16 v169, v18, v18\n"
    "v_dot2c_f32_bf16 v168, s64, v19\n"
    "v_dot2c_f32_bf16 v169, v19, v19\n"
    "v_dot2c_f32_bf16 v168, s64, v20\n"
    "v_dot2c_f32_bf16 v169, v20, v20\n"
    "v_dot2c_f32_bf16 v168, s64, v21\n"
    "s_add_u32 m0, s45, 17408\n"
    "s_add_u32 s51, s47, s84\n"
    "buffer_load_dwordx4 v3, s[24:27], s51 offen lds\n"
    "v_dot2c_f32_bf16 v169, v21, v21\n"
    "v_dot2c_f32_bf16 v168, s64, v22\n"
    "v_dot2c_f32_bf16 v169, v22, v22\n"
    "v_dot2c_f32_bf16 v168, s64, v23\n"
    "v_dot2c_f32_bf16 v169, v23, v23\n"
    "v_dot2c_f32_bf16 v168, s64, v24\n"
    "v_dot2c_f32_bf16 v169, v24, v24\n"
    "v_dot2c_f32_bf16 v168, s64, v25\n"
    "v_dot2c_f32_bf16 v169, v25, v25\n"
    "s_add_u32 m0, s45, 18432\n"
    "s_add_u32 s51, s48, s84\n"
    "buffer_load_dwordx4 v2, s[24:27], s51 offen lds\n"
    "v_dot2c_f32_bf16 v168, s64, v26\n"
    "v_dot2c_f32_bf16 v169, v26, v26\n"
    "v_dot2c_f32_bf16 v168, s64, v27\n"
    "v_dot2c_f32_bf16 v169, v27, v27\n"
    "v_dot2c_f32_bf16 v168, s64, v28\n"
    "v_dot2c_f32_bf16 v169, v28, v28\n"
    "v_dot2c_f32_bf16 v168, s64, v29\n"
    "v_dot2c_f32_bf16 v169, v29, v29\n"
    "v_dot2c_f32_bf16 v168, s64, v30\n"
    "s_add_u32 m0, s45, 19456\n"
    "s_add_u32 s51, s49, s84\n"
    "buffer_load_dwordx4 v3, s[24:27], s51 offen lds\n"
    "v_dot2c_f32_bf16 v169, v30, v30\n"
    "v_dot2c_f32_bf16 v168, s64, v31\n"
    "v_dot2c_f32_bf16 v169, v31, v31\n"
    "s_waitcnt vmcnt(8)\n"
    "ds_read_b128 v[48:51], v5 offset:8192\n"
    "ds_read_b128 v[52:55], v6 offset:8192\n"
    "ds_read_b128 v[56:59], v7 offset:8192\n"
    "ds_read_b128 v[60:63], v8 offset:8192\n"
    "s_waitcnt lgkmcnt(4)\n"
    "v_dot2c_f32_bf16 v168, s64, v32\n"
    "v_dot2c_f32_bf16 v169, v32, v32\n"
    "v_dot2c_f32_bf16 v168, s64, v33\n"
    "s_add_u32 m0, s45, 20480\n"
    "s_add_u32 s51, s46, s85\n"
    "buffer_load_dwordx4 v2, s[24:27], s51 offen lds\n"
    "v_dot2c_f32_bf16 v169, v33, v33\n"
    "v_dot2c_f32_bf16 v168, s64, v34\n"
    "v_dot2c_f32_bf16 v169, v34, v34\n"
    "v_dot2c_f32_bf16 v168, s64, v35\n"
    "v_dot2c_f32_bf16 v169, v35, v35\n"
    "v_dot2c_f32_bf16 v168, s64, v36\n"
    "v_dot2c_f32_bf16 v169, v36, v36\n"
    "v_dot2c_f32_bf16 v168, s64, v37\n"
    "v_dot2c_f32_bf16 v169, v37, v37\n"
    "s_add_u32 m0, s45, 21504\n"
    "s_add_u32 s51, s47, s85\n"
    "buffer_load_dwordx4 v3, s[24:27], s51 offen lds\n"
    "v_dot2c_f32_bf16 v168, s64, v38\n"
    "v_dot2c_f32_bf16 v169, v38, v38\n"
    "v_dot2c_f32_bf16 v168, s64, v39\n"
    "v_dot2c_f32_bf16 v169, v39, v39\n"
    "v_dot2c_f32_bf16 v168, s64, v40\n"
    "v_dot2c_f32_bf16 v169, v40, v40\n"
    "v_dot2c_f32_bf16 v168, s64, v41\n"
    "v_dot2c_f32_bf16 v169, v41, v41\n"
    "v_dot2c_f32_bf16 v168, s64, v42\n"
    "s_add_u32 m0, s45, 22528\n"
    "s_add_u32 s51, s48, s85\n"
    "buffer_load_dwordx4 v2, s[24:27], s51 offen lds\n"
    "v_dot2c_f32_bf16 v169, v42, v42\n"
    "v_dot2c_f32_bf16 v168, s64, v43\n"
    "v_dot2c_f32_bf16 v169, v43, v43\n"
    "v_dot2c_f32_bf16 v168, s64, v44\n"
    "v_dot2c_f32_bf16 v169, v44, v44\n"
    "v_dot2c_f32_bf16 v168, s64, v45\n"
    "v_dot2c_f32_bf16 v169, v45, v45\n"
    "v_dot2c_f32_bf16 v168, s64, v46\n"
    "v_dot2c_f32_bf16 v169, v46, v46\n"
    "s_add_u32 m0, s45, 23552\n"
    "s_add_u32 s51, s49, s85\n"
    "buffer_load_dwordx4 v3, s[24:27], s51 offen lds\n"
    "v_dot2c_f32_bf16 v168, s64, v47\n"
    "v_dot2c_f32_bf16 v169, v47, v47\n"
    "s_waitcnt vmcnt(8)\n"
    "ds_read_b128 v[64:67], v5 offset:12288\n"
    "ds_read_b128 v[68:71], v6 offset:12288\n"
    "ds_read_b128 v[72:75], v7 offset:12288\n"
    "ds_read_b128 v[76:79], v8 offset:12288\n"
    "s_waitcnt lgkmcnt(4)\n"
    "v_dot2c_f32_bf16 v168, s64, v48\n"
    "v_dot2c_f32_bf16 v169, v48, v48\n"
    "v_dot2c_f32_bf16 v168, s64, v49\n"
    "v_dot2c_f32_bf16 v169, v49, v49\n"
    "s_add_u32 m0, s45, 24576\n"
    "s_add_u32 s51, s46, s86\n"
    "buffer_load_dwordx4 v2, s[24:27], s51 offen lds\n"
    "v_dot2c_f32_bf16 v168, s64, v50\n"
    "v_dot2c_f32_bf16 v169, v50, v50\n"
    "v_dot2c_f32_bf16 v168, s64, v51\n"
    "v_dot2c_f32_bf16 v169, v51, v51\n"
    "v_dot2c_f32_bf16 v168, s64, v52\n"
    "v_dot2c_f32_bf16 v169, v52, v52\n"
    "v_dot2c_f32_bf16 v168, s64, v53\n"
    "v_dot2c_f32_bf16 v169, v53, v53\n"
    "v_dot2c_f32_bf16 v168, s64, v54\n"
    "s_add_u32 m0, s45, 25600\n"
    "s_add_u32 s51, s47, s86\n"
    "buffer_load_dwordx4 v3, s[24:27], s51 offen lds\n"
    "v_dot2c_f32_bf16 v169, v54, v54\n"
    "v_dot2c_f32_bf16 v168, s64, v55\n"
    "v_dot2c_f32_bf16 v169, v55, v55\n"
    "v_dot2c_f32_bf16 v168, s64, v56\n"
    "v_dot2c_f32_bf16 v169, v56, v56\n"
    "v_dot2c_f32_bf16 v168, s64, v57\n"
    "v_dot2c_f32_bf16 v169, v57, v57\n"
    "v_dot2c_f32_bf16 v168, s64, v58\n"
    "v_dot2c_f32_bf16 v169, v58, v58\n"
    "s_add_u32 m0, s45, 26624\n"
    "s_add_u32 s51, s48, s86\n"
    "buffer_load_dwordx4 v2, s[24:27], s51 offen lds\n"
    "v_dot2c_f32_bf16 v168, s64, v59\n"
    "v_dot2c_f32_bf16 v169, v59, v59\n"
    "v_dot2c_f32_bf16 v168, s64, v60\n"
    "v_dot2c_f32_bf16 v169, v60, v60\n"
    "v_dot2c_f32_bf16 v168, s64, v61\n"
    "v_dot2c_f32_bf16 v169, v61, v61\n"
    "v_dot2c_f32_bf16 v168, s64, v62\n"
    "v_dot2c_f32_bf16 v169, v62, v62\n"
    "v_dot2c_f32_bf16 v168, s64, v63\n"
    "s_add_u32 m0, s45, 27648\n"
    "s_add_u32 s51, s49, s86\n"
    "buffer_load_dwordx4 v3, s[24:27], s51 offen lds\n"
    "v_dot2c_f32_bf16 v169, v63, v63\n"
    "s_waitcnt vmcnt(8)\n"
    "ds_read_b128 v[80:83], v5 offset:16384\n"
    "ds_read_b128 v[84:87], v6 offset:16384\n"
    "ds_read_b128 v[88:91], v7 offset:16384\n"
    "ds_read_b128 v[92:95], v8 offset:16384\n"
    "s_waitcnt lgkmcnt(4)\n"
    "v_dot2c_f32_bf16 v168, s64, v64\n"
    "v_dot2c_f32_bf16 v169, v64, v64\n"
    "v_dot2c_f32_bf16 v168, s64, v65\n"
    "v_dot2c_f32_bf16 v169, v65, v65\n"
    "v_dot2c_f32_bf16 v168, s64, v66\n"
    "s_lshl_b32 s59, s83, 5\n"
    "s_add_u32 m0, s70, 126976\n"
    "s_add_u32 s51, s76, s59\n"
    "buffer_load_dwordx4 v1, s[36:39], s51 offen lds\n"
    "v_dot2c_f32_bf16 v169, v66, v66\n"
    "v_dot2c_f32_bf16 v168, s64, v67\n"
    "v_dot2c_f32_bf16 v169, v67, v67\n"
    "v_dot2c_f32_bf16 v168, s64, v68\n"
    "v_dot2c_f32_bf16 v169, v68, v68\n"
    "v_dot2c_f32_bf16 v168, s64, v69\n"
    "v_dot2c_f32_bf16 v169, v69, v69\n"
    "v_dot2c_f32_bf16 v168, s64, v70\n"
    "s_lshl_b32 s59, s84, 5\n"
    "s_add_u32 m0, s70, 131072\n"
    "s_add_u32 s51, s76, s59\n"
    "buffer_load_dwordx4 v1, s[36:39], s51 offen lds\n"
    "v_dot2c_f32_bf16 v169, v70, v70\n"
    "v_dot2c_f32_bf16 v168, s64, v71\n"
    "v_dot2c_f32_bf16 v169, v71, v71\n"
    "v_dot2c_f32_bf16 v168, s64, v72\n"
    "v_dot2c_f32_bf16 v169, v72, v72\n"
    "v_dot2c_f32_bf16 v168, s64, v73\n"
    "v_dot2c_f32_bf16 v169, v73, v73\n"
    "v_dot2c_f32_bf16 v168, s64, v74\n"
    "s_lshl_b32 s59, s85, 5\n"
    "s_add_u32 m0, s70, 135168\n"
    "s_add_u32 s51, s76, s59\n"
    "buffer_load_dwordx4 v1, s[36:39], s51 offen lds\n"
    "v_dot2c_f32_bf16 v169, v74, v74\n"
    "v_dot2c_f32_bf16 v168, s64, v75\n"
    "v_dot2c_f32_bf16 v169, v75, v75\n"
    "v_dot2c_f32_bf16 v168, s64, v76\n"
    "v_dot2c_f32_bf16 v169, v76, v76\n"
    "v_dot2c_f32_bf16 v168, s64, v77\n"
    "v_dot2c_f32_bf16 v169, v77, v77\n"
    "v_dot2c_f32_bf16 v168, s64, v78\n"
    "s_lshl_b32 s59, s86, 5\n"
    "s_add_u32 m0, s70, 139264\n"
    "s_add_u32 s51, s76, s59\n"
    "buffer_load_dwordx4 v1, s[36:39], s51 offen lds\n"
    "v_dot2c_f32_bf16 v169, v78, v78\n"
    "v_dot2c_f32_bf16 v168, s64, v79\n"
    "v_dot2c_f32_bf16 v169, v79, v79\n"
    "s_waitcnt vmcnt(8)\n"
    "ds_read_b128 v[96:99], v5 offset:20480\n"
    "ds_read_b128 v[100:103], v6 offset:20480\n"
    "ds_read_b128 v[104:107], v7 offset:20480\n"
    "ds_read_b128 v[108:111], v8 offset:20480\n"
    "s_waitcnt lgkmcnt(4)\n"
    "v_dot2c_f32_bf16 v168, s64, v80\n"
    "v_dot2c_f32_bf16 v169, v80, v80\n"
    "s_add_u32 m0, s45, 0\n"
    "s_add_u32 s23, s4, 0\n"
    "buffer_load_dwordx4 v4, s[28:31], s23 offen lds\n"
    "v_dot2c_f32_bf16 v168, s64, v81\n"
    "v_dot2c_f32_bf16 v169, v81, v81\n"
    "v_dot2c_f32_bf16 v168, s64, v82\n"
    "v_dot2c_f32_bf16 v169, v82, v82\n"
    "v_dot2c_f32_bf16 v168, s64, v83\n"
    "v_dot2c_f32_bf16 v169, v83, v83\n"
    "v_dot2c_f32_bf16 v168, s64, v84\n"
    "v_dot2c_f32_bf16 v169, v84, v84\n"
    "v_dot2c_f32_bf16 v168, s64, v85\n"
    "buffer_load_dwordx4 v252, s[28:31], s23 offen offset:1024 lds\n"
    "v_dot2c_f32_bf16 v169, v85, v85\n"
    "v_dot2c_f32_bf16 v168, s64, v86\n"
    "v_dot2c_f32_bf16 v169, v86, v86\n"
    "v_dot2c_f32_bf16 v168, s64, v87\n"
    "v_dot2c_f32_bf16 v169, v87, v87\n"
    "v_dot2c_f32_bf16 v168, s64, v88\n"
    "v_dot2c_f32_bf16 v169, v88, v88\n"
    "v_dot2c_f32_bf16 v168, s64, v89\n"
    "v_dot2c_f32_bf16 v169, v89, v89\n"
    "v_dot2c_f32_bf16 v168, s64, v90\n"
    "v_dot2c_f32_bf16 v169, v90, v90\n"
    "buffer_load_dwordx4 v253, s[28:31], s23 offen offset:2048 lds\n"
    "v_dot2c_f32_bf16 v168, s64, v91\n"
    "v_dot2c_f32_bf16 v169, v91, v91\n"
    "v_dot2c_f32_bf16 v168, s64, v92\n"
    "v_dot2c_f32_bf16 v169, v92, v92\n"
    "v_dot2c_f32_bf16 v168, s64, v93\n"
    "v_dot2c_f32_bf16 v169, v93, v93\n"
    "v_dot2c_f32_bf16 v168, s64, v94\n"
    "v_dot2c_f32_bf16 v169, v94, v94\n"
    "v_dot2c_f32_bf16 v168, s64, v95\n"
    "v_dot2c_f32_bf16 v169, v95, v95\n"
    "s_waitcnt vmcnt(7)\n"
    "ds_read_b128 v[112:115], v5 offset:24576\n"
    "buffer_load_dwordx4 v254, s[28:31], s23 offen offset:3072 lds\n"
    "ds_read_b128 v[116:119], v6 offset:24576\n"
    "ds_read_b128 v[120:123], v7 offset:24576\n"
    "ds_read_b128 v[124:127], v8 offset:24576\n"
    "s_waitcnt lgkmcnt(4)\n"
    "v_dot2c_f32_bf16 v168, s64, v96\n"
    "v_dot2c_f32_bf16 v169, v96, v96\n"
    "v_dot2c_f32_bf16 v168, s64, v97\n"
    "v_dot2c_f32_bf16 v169, v97, v97\n"
    "v_dot2c_f32_bf16 v168, s64, v98\n"
    "v_dot2c_f32_bf16 v169, v98, v98\n"
    "v_dot2c_f32_bf16 v168, s64, v99\n"
    "v_dot2c_f32_bf16 v169, v99, v99\n"
    "s_add_u32 m0, s45, 4096\n"
    "s_add_u32 s23, s5, 0\n"
    "buffer_load_dwordx4 v4, s[28:31], s23 offen lds\n"
    "v_dot2c_f32_bf16 v168, s64, v100\n"
    "v_dot2c_f32_bf16 v169, v100, v100\n"
    "v_dot2c_f32_bf16 v168, s64, v101\n"
    "v_dot2c_f32_bf16 v169, v101, v101\n"
    "v_dot2c_f32_bf16 v168, s64, v102\n"
    "v_dot2c_f32_bf16 v169, v102, v102\n"
    "v_dot2c_f32_bf16 v168, s64, v103\n"
    "v_dot2c_f32_bf16 v169, v103, v103\n"
    "v_dot2c_f32_bf16 v168, s64, v104\n"
    "buffer_load_dwordx4 v252, s[28:31], s23 offen offset:1024 lds\n"
    "v_dot2c_f32_bf16 v169, v104, v104\n"
    "v_dot2c_f32_bf16 v168, s64, v105\n"
    "v_dot2c_f32_bf16 v169, v105, v105\n"
    "v_dot2c_f32_bf16 v168, s64, v106\n"
    "v_dot2c_f32_bf16 v169, v106, v106\n"
    "v_dot2c_f32_bf16 v168, s64, v107\n"
    "v_dot2c_f32_bf16 v169, v107, v107\n"
    "v_dot2c_f32_bf16 v168, s64, v108\n"
    "v_dot2c_f32_bf16 v169, v108, v108\n"
    "v_dot2c_f32_bf16 v168, s64, v109\n"
    "v_dot2c_f32_bf16 v169, v109, v109\n"
    "buffer_load_dwordx4 v253, s[28:31], s23 offen offset:2048 lds\n"
    "v_dot2c_f32_bf16 v168, s64, v110\n"
    "v_dot2c_f32_bf16 v169, v110, v110\n"
    "v_dot2c_f32_bf16 v168, s64, v111\n"
    "v_dot2c_f32_bf16 v169, v111, v111\n"
    "s_waitcnt lgkmcnt(0)\n"
    "v_dot2c_f32_bf16 v168, s64, v112\n"
    "v_dot2c_f32_bf16 v169, v112, v112\n"
    "v_dot2c_f32_bf16 v168, s64, v113\n"
    "v_dot2c_f32_bf16 v169, v113, v113\n"
    "v_dot2c_f32_bf16 v168, s64, v114\n"
    "v_dot2c_f32_bf16 v169, v114, v114\n"
    "v_dot2c_f32_bf16 v168, s64, v115\n"
    "buffer_load_dwordx4 v254, s[28:31], s23 offen offset:3072 lds\n"
    "v_dot2c_f32_bf16 v169, v115, v115\n"
    "v_dot2c_f32_bf16 v168, s64, v116\n"
    "v_dot2c_f32_bf16 v169, v116, v116\n"
    "v_dot2c_f32_bf16 v168, s64, v117\n"
    "v_dot2c_f32_bf16 v169, v117, v117\n"
    "v_dot2c_f32_bf16 v168, s64, v118\n"
    "v_dot2c_f32_bf16 v169, v118, v118\n"
    "v_dot2c_f32_bf16 v168, s64, v119\n"
    "v_dot2c_f32_bf16 v169, v119, v119\n"
    "v_dot2c_f32_bf16 v168, s64, v120\n"
    "v_dot2c_f32_bf16 v169, v120, v120\n"
    "s_add_u32 m0, s45, 8192\n"
    "s_add_u32 s23, s6, 0\n"
    "buffer_load_dwordx4 v4, s[28:31], s23 offen lds\n"
    "v_dot2c_f32_bf16 v168, s64, v121\n"
    "v_dot2c_f32_bf16 v169, v121, v121\n"
    "v_dot2c_f32_bf16 v168, s64, v122\n"
    "v_dot2c_f32_bf16 v169, v122, v122\n"
    "v_dot2c_f32_bf16 v168, s64, v123\n"
    "v_dot2c_f32_bf16 v169, v123, v123\n"
    "v_dot2c_f32_bf16 v168, s64, v124\n"
    "v_dot2c_f32_bf16 v169, v124, v124\n"
    "v_dot2c_f32_bf16 v168, s64, v125\n"
    "buffer_load_dwordx4 v252, s[28:31], s23 offen offset:1024 lds\n"
    "v_dot2c_f32_bf16 v169, v125, v125\n"
    "v_dot2c_f32_bf16 v168, s64, v126\n"
    "v_dot2c_f32_bf16 v169, v126, v126\n"
    "v_dot2c_f32_bf16 v168, s64, v127\n"
    "v_dot2c_f32_bf16 v169, v127, v127\n"
    "s_nop 1\n"
    "v_mov_b32 v208, v168\n"
    "v_mov_b32 v209, v169\n"
    "v_xor_b32 v5, 0, v14\n"
    "v_permlane32_swap_b32 v208, v168\n"
    "v_permlane32_swap_b32 v209, v169\n"
    "v_add_f32 v208, v208, v168\n"
    "v_mul_f32 v208, 0x3b124925, v208\n"
    "v_add_f32 v209, v209, v169\n"
    "v_mul_f32 v209, 0x3b124925, v209\n"
    "v_fma_f32 v209, -v208, v208, v209\n"
    "v_max_f32 v209, 0, v209\n"
    "v_cvt_pk_bf16_f32 v210, v208, v209\n"
    "v_lshlrev_b32 v208, 16, v210\n"
    "v_xor_b32 v192, 0x80000000, v208\n"
    "v_mov_b32 v193, v192\n"
    "v_mov_b32 v194, v192\n"
    "v_mov_b32 v195, v192\n"
    "v_mov_b32 v196, v192\n"
    "v_mov_b32 v197, v192\n"
    "buffer_load_dwordx4 v253, s[28:31], s23 offen offset:2048 lds\n"
    "v_mov_b32 v198, v192\n"
    "v_mov_b32 v199, v192\n"
    "v_mov_b32 v200, v192\n"
    "v_mov_b32 v201, v192\n"
    "v_mov_b32 v202, v192\n"
    "v_mov_b32 v203, v192\n"
    "v_mov_b32 v204, v192\n"
    "v_mov_b32 v205, v192\n"
    "v_mov_b32 v206, v192\n"
    "v_mov_b32 v207, v192\n"
    "; ---- x - mean through the matrix pipe: exact in f32, rounded once to bf16 ----\n"
    "s_nop 1\n"
    "v_mfma_f32_32x32x16_bf16 v[160:175], a[0:3], v[16:19], v[192:207]\n"
    "v_and_b32 v209, s77, v210\n"
    "v_add_f32 v209, 0x3727c5ac, v209\n"
    "buffer_load_dwordx4 v254, s[28:31], s23 offen offset:3072 lds\n"
    "v_cvt_pk_bf16_f32 v209, v209, v209\n"
    "v_and_b32 v209, s77, v209\n"
    "v_mfma_f32_32x32x16_bf16 v[160:175], a[4:7], v[20:23], v[160:175]\n"
    "v_sqrt_f32 v210, v209\n"
    "v_xor_b32 v6, 32, v14\n"
    "v_cvt_pk_bf16_f32 v210, v210, v210\n"
    "v_and_b32 v210, s77, v210\n"
    "v_mfma_f32_32x32x16_bf16 v[176:191], a[0:3], v[24:27], v[192:207]\n"
    "s_add_u32 m0, s45, 12288\n"
    "s_add_u32 s23, s7, 0\n"
    "buffer_load_dwordx4 v4, s[28:31], s23 offen lds\n"
    "v_rcp_f32 v211, v210\n"
    "v_xor_b32 v7, 64, v14\n"
    "v_xor_b32 v8, 96, v14\n"
    "v_mul_f32 v15, v210, v210\n"
    "v_mfma_f32_32x32x16_bf16 v[176:191], a[4:7], v[28:31], v[176:191]\n"
    "v_mul_f32 v14, 0xbfb8aa3b, v211\n"
    "v_mfma_f32_32x32x16_bf16 v[144:159], a[0:3], v[32:35], v[192:207]\n"
    "buffer_load_dwordx4 v252, s[28:31], s23 offen offset:1024 lds\n"
    "v_cvt_pk_bf16_f32 v16, v160, v161\n"
    "v_cvt_pk_bf16_f32 v17, v162, v163\n"
    "v_cvt_pk_bf16_f32 v18, v164, v165\n"
    "v_cvt_pk_bf16_f32 v19, v166, v167\n"
    "v_mfma_f32_32x32x16_bf16 v[144:159], a[4:7], v[36:39], v[144:159]\n"
    "v_cvt_pk_bf16_f32 v20, v168, v169\n"
    "v_cvt_pk_bf16_f32 v21, v170, v171\n"
    "v_cvt_pk_bf16_f32 v22, v172, v173\n"
    "v_cvt_pk_bf16_f32 v23, v174, v175\n"
    "v_mfma_f32_32x32x16_bf16 v[160:175], a[0:3], v[40:43], v[192:207]\n"
    "buffer_load_dwordx4 v253, s[28:31], s23 offen offset:2048 lds\n"
    "v_cvt_pk_bf16_f32 v24, v176, v177\n"
    "v_cvt_pk_bf16_f32 v25, v178, v179\n"
    "v_cvt_pk_bf16_f32 v26, v180, v181\n"
    "v_cvt_pk_bf16_f32 v27, v182, v183\n"
    "v_mfma_f32_32x32x16_bf16 v[160:175], a[4:7], v[44:47], v[160:175]\n"
    "v_cvt_pk_bf16_f32 v28, v184, v185\n"
    "v_cvt_pk_bf16_f32 v29, v186, v187\n"
    "v_cvt_pk_bf16_f32 v30, v188, v189\n"
    "v_cvt_pk_bf16_f32 v31, v190, v191\n"
    "v_mfma_f32_32x32x16_bf16 v[176:191], a[0:3], v[48:51], v[192:207]\n"
    "buffer_load_dwordx4 v254, s[28:31], s23 offen offset:3072 lds\n"
    "v_cvt_pk_bf16_f32 v32, v144, v145\n"
    "v_cvt_pk_bf16_f32 v33, v146, v147\n"
    "v_cvt_pk_bf16_f32 v34, v148, v149\n"
    "v_cvt_pk_bf16_f32 v35, v150, v151\n"
    "v_mfma_f32_32x32x16_bf16 v[176:191], a[4:7], v[52:55], v[176:191]\n"
    "v_cvt_pk_bf16_f32 v36, v152, v153\n"
    "v_cvt_pk_bf16_f32 v37, v154, v155\n"
    "v_cvt_pk_bf16_f32 v38, v156, v157\n"
    "v_cvt_pk_bf16_f32 v39, v158, v159\n"
    "v_mfma_f32_32x32x16_bf16 v[144:159], a[0:3], v[56:59], v[192:207]\n"
    "s_add_u32 m0, s45, 16384\n"
    "s_add_u32 s23, s8, 0\n"
    "buffer_load_dwordx4 v4, s[28:31], s23 offen lds\n"
    "v_cvt_pk_bf16_f32 v40, v160, v161\n"
    "v_cvt_pk_bf16_f32 v41, v162, v163\n"
    "v_cvt_pk_bf16_f32 v42, v164, v165\n"
    "v_cvt_pk_bf16_f32 v43, v166, v167\n"
    "v_mfma_f32_32x32x16_bf16 v[144:159], a[4:7], v[60:63], v[144:159]\n"
    "v_cvt_pk_bf16_f32 v44, v168, v169\n"
    "v_cvt_pk_bf16_f32 v45, v170, v171\n"
    "v_cvt_pk_bf16_f32 v46, v172, v173\n"
    "v_cvt_pk_bf16_f32 v47, v174, v175\n"
    "v_mfma_f32_32x32x16_bf16 v[160:175], a[0:3], v[64:67], v[192:207]\n"
    "buffer_load_dwordx4 v252, s[28:31], s23 offen offset:1024 lds\n"
    "v_cvt_pk_bf16_f32 v48, v176, v177\n"
    "v_cvt_pk_bf16_f32 v49, v178, v179\n"
    "v_cvt_pk_bf16_f32 v50, v180, v181\n"
    "v_cvt_pk_bf16_f32 v51, v182, v183\n"
    "v_mfma_f32_32x32x16_bf16 v[160:175], a[4:7], v[68:71], v[160:175]\n"
    "v_cvt_pk_bf16_f32 v52, v184, v185\n"
    "v_cvt_pk_bf16_f32 v53, v186, v187\n"
    "v_cvt_pk_bf16_f32 v54, v188, v189\n"
    "v_cvt_pk_bf16_f32 v55, v190, v191\n"
    "v_mfma_f32_32x32x16_bf16 v[176:191], a[0:3], v[72:75], v[192:207]\n"
    "buffer_load_dwordx4 v253, s[28:31], s23 offen offset:2048 lds\n"
    "v_cvt_pk_bf16_f32 v56, v144, v145\n"
    "v_cvt_pk_bf16_f32 v57, v146, v147\n"
    "v_cvt_pk_bf16_f32 v58, v148, v149\n"
    "v_cvt_pk_bf16_f32 v59, v150, v151\n"
    "v_mfma_f32_32x32x16_bf16 v[176:191], a[4:7], v[76:79], v[176:191]\n"
    "v_cvt_pk_bf16_f32 v60, v152, v153\n"
    "v_cvt_pk_bf16_f32 v61, v154, v155\n"
    "v_cvt_pk_bf16_f32 v62, v156, v157\n"
    "v_cvt_pk_bf16_f32 v63, v158, v159\n"
    "v_mfma_f32_32x32x16_bf16 v[144:159], a[0:3], v[80:83], v[192:207]\n"
    "buffer_load_dwordx4 v254, s[28:31], s23 offen offset:3072 lds\n"
    "v_cvt_pk_bf16_f32 v64, v160, v161\n"
    "v_cvt_pk_bf16_f32 v65, v162, v163\n"
    "v_cvt_pk_bf16_f32 v66, v164, v165\n"
    "v_cvt_pk_bf16_f32 v67, v166, v167\n"
    "v_mfma_f32_32x32x16_bf16 v[144:159], a[4:7], v[84:87], v[144:159]\n"
    "v_cvt_pk_bf16_f32 v68, v168, v169\n"
    "v_cvt_pk_bf16_f32 v69, v170, v171\n"
    "v_cvt_pk_bf16_f32 v70, v172, v173\n"
    "v_cvt_pk_bf16_f32 v71, v174, v175\n"
    "v_mfma_f32_32x32x16_bf16 v[160:175], a[0:3], v[88:91], v[192:207]\n"
    "s_add_u32 m0, s45, 20480\n"
    "s_add_u32 s23, s9, 0\n"
    "buffer_load_dwordx4 v4, s[28:31], s23 offen lds\n"
    "v_cvt_pk_bf16_f32 v72, v176, v177\n"
    "v_cvt_pk_bf16_f32 v73, v178, v179\n"
    "v_cvt_pk_bf16_f32 v74, v180, v181\n"
    "v_cvt_pk_bf16_f32 v75, v182, v183\n"
    "v_mfma_f32_32x32x16_bf16 v[160:175], a[4:7], v[92:95], v[160:175]\n"
    "v_cvt_pk_bf16_f32 v76, v184, v185\n"
    "v_cvt_pk_bf16_f32 v77, v186, v187\n"
    "v_cvt_pk_bf16_f32 v78, v188, v189\n"
    "v_cvt_pk_bf16_f32 v79, v190, v191\n"
    "v_mfma_f32_32x32x16_bf16 v[176:191], a[0:3], v[96:99], v[192:207]\n"
    "buffer_load_dwordx4 v252, s[28:31], s23 offen offset:1024 lds\n"
    "v_cvt_pk_bf16_f32 v80, v144, v145\n"
    "v_cvt_pk_bf16_f32 v81, v146, v147\n"
    "v_cvt_pk_bf16_f32 v82, v148, v149\n"
    "v_cvt_pk_bf16_f32 v83, v150, v151\n"
    "v_mfma_f32_32x32x16_bf16 v[176:191], a[4:7], v[100:103], v[176:191]\n"
    "v_cvt_pk_bf16_f32 v84, v152, v153\n"
    "v_cvt_pk_bf16_f32 v85, v154, v155\n"
    "v_cvt_pk_bf16_f32 v86, v156, v157\n"
    "v_cvt_pk_bf16_f32 v87, v158, v159\n"
    "v_mfma_f32_32x32x16_bf16 v[144:159], a[0:3], v[104:107], v[192:207]\n"
    "buffer_load_dwordx4 v253, s[28:31], s23 offen offset:2048 lds\n"
    "v_cvt_pk_bf16_f32 v88, v160, v161\n"
    "v_cvt_pk_bf16_f32 v89, v162, v163\n"
    "v_cvt_pk_bf16_f32 v90, v164, v165\n"
    "v_cvt_pk_bf16_f32 v91, v166, v167\n"
    "v_mfma_f32_32x32x16_bf16 v[144:159], a[4:7], v[108:111], v[144:159]\n"
    "v_cvt_pk_bf16_f32 v92, v168, v169\n"
    "v_cvt_pk_bf16_f32 v93, v170, v171\n"
    "v_cvt_pk_bf16_f32 v94, v172, v173\n"
    "v_cvt_pk_bf16_f32 v95, v174, v175\n"
    "v_mfma_f32_32x32x16_bf16 v[160:175], a[0:3], v[112:115], v[192:207]\n"
    "buffer_load_dwordx4 v254, s[28:31], s23 offen offset:3072 lds\n"
    "v_cvt_pk_bf16_f32 v96, v176, v177\n"
    "v_cvt_pk_bf16_f32 v97, v178, v179\n"
    "v_cvt_pk_bf16_f32 v98, v180, v181\n"
    "v_cvt_pk_bf16_f32 v99, v182, v183\n"
    "v_mfma_f32_32x32x16_bf16 v[160:175], a[4:7], v[116:119], v[160:175]\n"
    "v_cvt_pk_bf16_f32 v100, v184, v185\n"
    "v_cvt_pk_bf16_f32 v101, v186, v187\n"
    "v_cvt_pk_bf16_f32 v102, v188, v189\n"
    "v_cvt_pk_bf16_f32 v103, v190, v191\n"
    "v_mfma_f32_32x32x16_bf16 v[176:191], a[0:3], v[120:123], v[192:207]\n"
    "s_add_u32 m0, s45, 24576\n"
    "s_add_u32 s23, s10, 0\n"
    "buffer_load_dwordx4 v4, s[28:31], s23 offen lds\n"
    "v_cvt_pk_bf16_f32 v104, v144, v145\n"
    "v_cvt_pk_bf16_f32 v105, v146, v147\n"
    "v_cvt_pk_bf16_f32 v106, v148, v149\n"
    "v_cvt_pk_bf16_f32 v107, v150, v151\n"
    "v_mfma_f32_32x32x16_bf16 v[176:191], a[4:7], v[124:127], v[176:191]\n"
    "v_cvt_pk_bf16_f32 v108, v152, v153\n"
    "v_cvt_pk_bf16_f32 v109, v154, v155\n"
    "v_cvt_pk_bf16_f32 v110, v156, v157\n"
    "v_cvt_pk_bf16_f32 v111, v158, v159\n"
    "s_barrier\n"
    "ds_read_b128 a[224:227], v13 offset:0\n"
    "ds_read_b128 a[228:231], v13 offset:1024\n"
    "ds_read_b128 a[232:235], v13 offset:2048\n"
    "ds_read_b128 a[236:239], v13 offset:3072\n"
    "ds_read_b128 a[240:243], v13 offset:4096\n"
    "ds_read_b128 a[244:247], v13 offset:5120\n"
    "s_waitcnt lgkmcnt(2)\n"
    "v_mfma_f32_32x32x16_bf16 v[128:143], v[16:19], a[224:227], 0\n"
    "ds_read_b128 a[248:251], v13 offset:6144\n"
    "buffer_load_dwordx4 v252, s[28:31], s23 offen offset:1024 lds\n"
    "v_cvt_pk_bf16_f32 v112, v160, v161\n"
    "v_cvt_pk_bf16_f32 v113, v162, v163\n"
    "v_cvt_pk_bf16_f32 v114, v164, v165\n"
    "v_mfma_f32_32x32x16_bf16 v[128:143], v[20:23], a[228:231], v[128:143]\n"
    "ds_read_b128 a[252:255], v13 offset:7168\n"
    "v_cvt_pk_bf16_f32 v115, v166, v167\n"
    "v_cvt_pk_bf16_f32 v116, v168, v169\n"
    "v_cvt_pk_bf16_f32 v117, v170, v171\n"
    "v_cvt_pk_bf16_f32 v118, v172, v173\n"
    "v_mfma_f32_32x32x16_bf16 v[128:143], v[24:27], a[232:235], v[128:143]\n"
    "ds_read_b128 a[224:227], v13 offset:8192\n"
    "buffer_load_dwordx4 v253, s[28:31], s23 offen offset:2048 lds\n"
    "v_cvt_pk_bf16_f32 v119, v174, v175\n"
    "v_cvt_pk_bf16_f32 v120, v176, v177\n"
    "v_cvt_pk_bf16_f32 v121, v178, v179\n"
    "v_mfma_f32_32x32x16_bf16 v[128:143], v[28:31], a[236:239], v[128:143]\n"
    "ds_read_b128 a[228:231], v13 offset:9216\n"
    "v_cvt_pk_bf16_f32 v122, v180, v181\n"
    "v_cvt_pk_bf16_f32 v123, v182, v183\n"
    "v_cvt_pk_bf16_f32 v124, v184, v185\n"
    "v_cvt_pk_bf16_f32 v125, v186, v187\n"
    "s_waitcnt lgkmcnt(2)\n"
    "v_mfma_f32_32x32x16_bf16 v[128:143], v[32:35], a[240:243], v[128:143]\n"
    "ds_read_b128 a[232:235], v13 offset:10240\n"
    "buffer_load_dwordx4 v254, s[28:31], s23 offen offset:3072 lds\n"
    "v_cvt_pk_bf16_f32 v126, v188, v189\n"
    "v_cvt_pk_bf16_f32 v127, v190, v191\n"
    "s_lshr_b32 s59, s44, 1\n"
    "v_mfma_f32_32x32x16_bf16 v[128:143], v[36:39], a[244:247], v[128:143]\n"
    "ds_read_b128 a[236:239], v13 offset:11264\n"
    "s_lshl_b32 s59, s59, 10\n"
    "v_add_u32 v2, s59, v1\n"
    "v_xor_b32 v3, 0x400, v2\n"
    "ds_write_b32 v248, v211\n"
    "v_mfma_f32_32x32x16_bf16 v[128:143], v[40:43], a[248:251], v[128:143]\n"
    "ds_read_b128 a[240:243], v13 offset:12288\n"
    "s_add_u32 m0, s71, 147456\n"
    "s_add_u32 s23, s6, 128\n"
    "buffer_load_dwordx4 v4, s[28:31], s23 offen lds\n"
    "ds_read_b128 v[192:195], v249 offset:0\n"
    "ds_read_b128 v[196:199], v249 offset:32\n"
    "ds_read_b128 v[200:203], v249 offset:64\n"
    "v_mfma_f32_32x32x16_bf16 v[128:143], v[44:47], a[252:255], v[128:143]\n"
    "ds_read_b128 a[244:247], v13 offset:13312\n"
    "ds_read_b128 v[204:207], v249 offset:96\n"
    "s_waitcnt lgkmcnt(7)\n"
    "v_mfma_f32_32x32x16_bf16 v[128:143], v[48:51], a[224:227], v[128:143]\n"
    "ds_read_b128 a[248:251], v13 offset:14336\n"
    "buffer_load_dwordx4 v252, s[28:31], s23 offen offset:1024 lds\n"
    "v_mfma_f32_32x32x16_bf16 v[128:143], v[52:55], a[228:231], v[128:143]\n"
    "ds_read_b128 a[252:255], v13 offset:15360\n"
    "v_mfma_f32_32x32x16_bf16 v[128:143], v[56:59], a[232:235], v[128:143]\n"
    "ds_read_b128 a[224:227], v13 offset:16384\n"
    "buffer_load_dwordx4 v253, s[28:31], s23 offen offset:2048 lds\n"
    "v_mfma_f32_32x32x16_bf16 v[128:143], v[60:63], a[236:239], v[128:143]\n"
    "ds_read_b128 a[228:231], v13 offset:17408\n"
    "s_waitcnt lgkmcnt(2)\n"
    "v_mfma_f32_32x32x16_bf16 v[128:143], v[64:67], a[240:243], v[128:143]\n"
    "ds_read_b128 a[232:235], v13 offset:18432\n"
    "buffer_load_dwordx4 v254, s[28:31], s23 offen offset:3072 lds\n"
    "v_mfma_f32_32x32x16_bf16 v[128:143], v[68:71], a[244:247], v[128:143]\n"
    "ds_read_b128 a[236:239], v13 offset:19456\n"
    "v_mfma_f32_32x32x16_bf16 v[128:143], v[72:75], a[248:251], v[128:143]\n"
    "ds_read_b128 a[240:243], v13 offset:20480\n"
    "v_mfma_f32_32x32x16_bf16 v[128:143], v[76:79], a[252:255], v[128:143]\n"
    "ds_read_b128 a[244:247], v13 offset:21504\n"
    "s_waitcnt lgkmcnt(2)\n"
    "v_mfma_f32_32x32x16_bf16 v[128:143], v[80:83], a[224:227], v[128:143]\n"
    "ds_read_b128 a[248:251], v13 offset:22528\n"
    "v_mfma_f32_32x32x16_bf16 v[128:143], v[84:87], a[228:231], v[128:143]\n"
    "ds_read_b128 a[252:255], v13 offset:23552\n"
    "v_mfma_f32_32x32x16_bf16 v[128:143], v[88:91], a[232:235], v[128:143]\n"
    "ds_read_b128 a[224:227], v13 offset:24576\n"
    "v_mfma_f32_32x32x16_bf16 v[128:143], v[92:95], a[236:239], v[128:143]\n"
    "ds_read_b128 a[228:231], v13 offset:25600\n"
    "s_waitcnt lgkmcnt(2)\n"
    "v_mfma_f32_32x32x16_bf16 v[128:143], v[96:99], a[240:243], v[128:143]\n"
    "ds_read_b128 a[232:235], v13 offset:26624\n"
    "v_mfma_f32_32x32x16_bf16 v[128:143], v[100:103], a[244:247], v[128:143]\n"
    "ds_read_b128 a[236:239], v13 offset:27648\n"
    "v_mfma_f32_32x32x16_bf16 v[128:143], v[104:107], a[248:251], v[128:143]\n"
    "s_waitcnt vmcnt(16)\n"
    "s_barrier\n"
    "ds_read_b64_tr_b16 a[240:241], v9 offset:0\n"
    "ds_read_b64_tr_b16 a[242:243], v9 offset:1024\n"
    "v_mfma_f32_32x32x16_bf16 v[128:143], v[108:111], a[252:255], v[128:143]\n"
    "ds_read_b64_tr_b16 a[244:245], v9 offset:2048\n"
    "ds_read_b64_tr_b16 a[246:247], v9 offset:3072\n"
    "s_waitcnt lgkmcnt(4)\n"
    "v_mfma_f32_32x32x16_bf16 v[128:143], v[112:115], a[224:227], v[128:143]\n"
    "ds_read_b64_tr_b16 a[248:249], v9 offset:28672\n"
    "ds_read_b64_tr_b16 a[250:251], v9 offset:29696\n"
    "v_mfma_f32_32x32x16_bf16 v[128:143], v[116:119], a[228:231], v[128:143]\n"
    "ds_read_b64_tr_b16 a[252:253], v9 offset:30720\n"
    "ds_read_b64_tr_b16 a[254:255], v9 offset:31744\n"
    "v_mfma_f32_32x32x16_bf16 v[128:143], v[120:123], a[232:235], v[128:143]\n"
    "ds_read_b64_tr_b16 a[224:225], v9 offset:4096\n"
    "ds_read_b64_tr_b16 a[226:227], v9 offset:5120\n"
    "v_mfma_f32_32x32x16_bf16 v[128:143], v[124:127], a[236:239], v[128:143]\n"
    "ds_read_b64_tr_b16 a[228:229], v9 offset:6144\n"
    "ds_read_b64_tr_b16 a[230:231], v9 offset:7168\n"
    "s_waitcnt lgkmcnt(4)\n"
    "v_mfma_f32_32x32x16_bf16 v[144:159], a[240:243], v[16:19], 0\n"
    "ds_read_b64_tr_b16 a[232:233], v9 offset:32768\n"
    "ds_read_b64_tr_b16 a[234:235], v9 offset:33792\n"
    "v_mfma_f32_32x32x16_bf16 v[144:159], a[244:247], v[20:23], v[144:159]\n"
    "ds_read_b64_tr_b16 a[236:237], v9 offset:34816\n"
    "ds_read_b64_tr_b16 a[238:239], v9 offset:35840\n"
    "v_mfma_f32_32x32x16_bf16 v[144:159], a[248:251], v[24:27], v[144:159]\n"
    "ds_read_b64_tr_b16 a[240:241], v9 offset:8192\n"
    "ds_read_b64_tr_b16 a[242:243], v9 offset:9216\n"
    "v_mul_f32 v128, v192, v128\n"
    "v_mul_f32 v129, v193, v129\n"
    "v_mul_f32 v130, v194, v130\n"
    "v_mfma_f32_32x32x16_bf16 v[144:159], a[252:255], v[28:31], v[144:159]\n"
    "ds_read_b64_tr_b16 a[244:245], v9 offset:10240\n"
    "ds_read_b64_tr_b16 a[246:247], v9 offset:11264\n"
    "v_mul_f32 v131, v195, v131\n"
    "v_mul_f32 v132, v196, v132\n"
    "v_mul_f32 v133, v197, v133\n"
    "s_waitcnt lgkmcnt(4)\n"
    "v_mfma_f32_32x32x16_bf16 v[144:159], a[224:227], v[32:35], v[144:159]\n"
    "ds_read_b64_tr_b16 a[248:249], v9 offset:36864\n"
    "ds_read_b64_tr_b16 a[250:251], v9 offset:37888\n"
    "v_mul_f32 v134, v198, v134\n"
    "v_mul_f32 v135, v199, v135\n"
    "v_mul_f32 v136, v200, v136\n"
    "v_mfma_f32_32x32x16_bf16 v[144:159], a[228:231], v[36:39], v[144:159]\n"
    "ds_read_b64_tr_b16 a[252:253], v9 offset:38912\n"
    "ds_read_b64_tr_b16 a[254:255], v9 offset:39936\n"
    "v_mul_f32 v137, v201, v137\n"
    "v_mul_f32 v138, v202, v138\n"
    "v_mul_f32 v139, v203, v139\n"
    "v_mfma_f32_32x32x16_bf16 v[144:159], a[232:235], v[40:43], v[144:159]\n"
    "ds_read_b64_tr_b16 a[224:225], v9 offset:12288\n"
    "ds_read_b64_tr_b16 a[226:227], v9 offset:13312\n"
    "v_mul_f32 v140, v204, v140\n"
    "v_mul_f32 v141, v205, v141\n"
    "v_mul_f32 v142, v206, v142\n"
    "v_mfma_f32_32x32x16_bf16 v[144:159], a[236:239], v[44:47], v[144:159]\n"
    "ds_read_b64_tr_b16 a[228:229], v9 offset:14336\n"
    "ds_read_b64_tr_b16 a[230:231], v9 offset:15360\n"
    "v_mul_f32 v143, v207, v143\n"
    "v_cvt_pk_bf16_f32 v128, v128, v129\n"
    "v_cvt_pk_bf16_f32 v129, v130, v131\n"
    "s_waitcnt lgkmcnt(4)\n"
    "v_mfma_f32_32x32x16_bf16 v[144:159], a[240:243], v[48:51], v[144:159]\n"
    "ds_read_b64_tr_b16 a[232:233], v9 offset:40960\n"
    "ds_read_b64_tr_b16 a[234:235], v9 offset:41984\n"
    "v_cvt_pk_bf16_f32 v130, v132, v133\n"
    "v_cvt_pk_bf16_f32 v131, v134, v135\n"
    "v_cvt_pk_bf16_f32 v132, v136, v137\n"
    "v_mfma_f32_32x32x16_bf16 v[144:159], a[244:247], v[52:55], v[144:159]\n"
    "ds_read_b64_tr_b16 a[236:237], v9 offset:43008\n"
    "ds_read_b64_tr_b16 a[238:239], v9 offset:44032\n"
    "v_cvt_pk_bf16_f32 v133, v138, v139\n"
    "v_cvt_pk_bf16_f32 v134, v140, v141\n"
    "v_cvt_pk_bf16_f32 v135, v142, v143\n"
    "v_mfma_f32_32x32x16_bf16 v[144:159], a[248:251], v[56:59], v[144:159]\n"
    "s_waitcnt vmcnt(4)\n"
    "s_barrier\n"
    "ds_read_b64_tr_b16 a[240:241], v9 offset:16384\n"
    "ds_read_b64_tr_b16 a[242:243], v9 offset:17408\n"
    "v_permlane16_swap_b32 v128, v132\n"
    "v_permlane16_swap_b32 v129, v133\n"
    "v_permlane16_swap_b32 v130, v134\n"
    "v_mfma_f32_32x32x16_bf16 v[144:159], a[252:255], v[60:63], v[144:159]\n"
    "ds_read_b64_tr_b16 a[244:245], v9 offset:18432\n"
    "ds_read_b64_tr_b16 a[246:247], v9 offset:19456\n"
    "s_add_u32 m0, s71, 114688\n"
    "s_add_u32 s23, s4, 128\n"
    "buffer_load_dwordx4 v4, s[28:31], s23 offen lds\n"
    "v_permlane16_swap_b32 v131, v135\n"
    "s_waitcnt lgkmcnt(4)\n"
    "v_mfma_f32_32x32x16_bf16 v[144:159], a[224:227], v[64:67], v[144:159]\n"
    "ds_read_b64_tr_b16 a[248:249], v9 offset:45056\n"
    "ds_read_b64_tr_b16 a[250:251], v9 offset:46080\n"
    "buffer_store_dwordx4 v[128:131], v220, s[32:35], s65 offen sc1\n"
    "s_add_u32 s65, s65, 65536\n"
    "buffer_store_dwordx4 v[132:135], v220, s[32:35], s65 offen sc1\n"
    "v_mfma_f32_32x32x16_bf16 v[144:159], a[228:231], v[68:71], v[144:159]\n"
    "ds_read_b64_tr_b16 a[252:253], v9 offset:47104\n"
    "ds_read_b64_tr_b16 a[254:255], v9 offset:48128\n"
    "buffer_load_dwordx4 v252, s[28:31], s23 offen offset:1024 lds\n"
    "v_mfma_f32_32x32x16_bf16 v[144:159], a[232:235], v[72:75], v[144:159]\n"
    "ds_read_b64_tr_b16 a[224:225], v9 offset:20480\n"
    "ds_read_b64_tr_b16 a[226:227], v9 offset:21504\n"
    "v_mfma_f32_32x32x16_bf16 v[144:159], a[236:239], v[76:79], v[144:159]\n"
    "ds_read_b64_tr_b16 a[228:229], v9 offset:22528\n"
    "ds_read_b64_tr_b16 a[230:231], v9 offset:23552\n"
    "buffer_load_dwordx4 v253, s[28:31], s23 offen offset:2048 lds\n"
    "s_waitcnt lgkmcnt(4)\n"
    "v_mfma_f32_32x32x16_bf16 v[144:159], a[240:243], v[80:83], v[144:159]\n"
    "ds_read_b64_tr_b16 a[232:233], v9 offset:49152\n"
    "ds_read_b64_tr_b16 a[234:235], v9 offset:50176\n"
    "v_mfma_f32_32x32x16_bf16 v[144:159], a[244:247], v[84:87], v[144:159]\n"
    "ds_read_b64_tr_b16 a[236:237], v9 offset:51200\n"
    "ds_read_b64_tr_b16 a[238:239], v9 offset:52224\n"
    "buffer_load_dwordx4 v254, s[28:31], s23 offen offset:3072 lds\n"
    "v_mfma_f32_32x32x16_bf16 v[144:159], a[248:251], v[88:91], v[144:159]\n"
    "ds_read_b64_tr_b16 a[240:241], v9 offset:24576\n"
    "ds_read_b64_tr_b16 a[242:243], v9 offset:25600\n"
    "v_mfma_f32_32x32x16_bf16 v[144:159], a[252:255], v[92:95], v[144:159]\n"
    "ds_read_b64_tr_b16 a[244:245], v9 offset:26624\n"
    "ds_read_b64_tr_b16 a[246:247], v9 offset:27648\n"
    "s_add_u32 m0, s71, 131072\n"
    "s_add_u32 s23, s5, 128\n"
    "buffer_load_dwordx4 v4, s[28:31], s23 offen lds\n"
    "s_waitcnt lgkmcnt(4)\n"
    "v_mfma_f32_32x32x16_bf16 v[144:159], a[224:227], v[96:99], v[144:159]\n"
    "ds_read_b64_tr_b16 a[248:249], v9 offset:53248\n"
    "ds_read_b64_tr_b16 a[250:251], v9 offset:54272\n"
    "v_mfma_f32_32x32x16_bf16 v[144:159], a[228:231], v[100:103], v[144:159]\n"
    "ds_read_b64_tr_b16 a[252:253], v9 offset:55296\n"
    "ds_read_b64_tr_b16 a[254:255], v9 offset:56320\n"
    "buffer_load_dwordx4 v252, s[28:31], s23 offen offset:1024 lds\n"
    "v_mfma_f32_32x32x16_bf16 v[144:159], a[232:235], v[104:107], v[144:159]\n"
    "ds_read_b64_tr_b16 a[224:225], v10 offset:0\n"
    "ds_read_b64_tr_b16 a[226:227], v10 offset:1024\n"
    "v_mfma_f32_32x32x16_bf16 v[144:159], a[236:239], v[108:111], v[144:159]\n"
    "ds_read_b64_tr_b16 a[228:229], v10 offset:2048\n"
    "ds_read_b64_tr_b16 a[230:231], v10 offset:3072\n"
    "buffer_load_dwordx4 v253, s[28:31], s23 offen offset:2048 lds\n"
    "s_waitcnt lgkmcnt(4)\n"
    "v_mfma_f32_32x32x16_bf16 v[144:159], a[240:243], v[112:115], v[144:159]\n"
    "ds_read_b64_tr_b16 a[232:233], v10 offset:28672\n"
    "ds_read_b64_tr_b16 a[234:235], v10 offset:29696\n"
    "v_mfma_f32_32x32x16_bf16 v[144:159], a[244:247], v[116:119], v[144:159]\n"
    "ds_read_b64_tr_b16 a[236:237], v10 offset:30720\n"
    "ds_read_b64_tr_b16 a[238:239], v10 offset:31744\n"
    "buffer_load_dwordx4 v254, s[28:31], s23 offen offset:3072 lds\n"
    "v_mfma_f32_32x32x16_bf16 v[144:159], a[248:251], v[120:123], v[144:159]\n"
    "ds_read_b64_tr_b16 a[240:241], v10 offset:4096\n"
    "ds_read_b64_tr_b16 a[242:243], v10 offset:5120\n"
    "v_mfma_f32_32x32x16_bf16 v[144:159], a[252:255], v[124:127], v[144:159]\n"
    "ds_read_b64_tr_b16 a[244:245], v10 offset:6144\n"
    "ds_read_b64_tr_b16 a[246:247], v10 offset:7168\n"
    "s_waitcnt lgkmcnt(4)\n"
    "v_mfma_f32_32x32x16_bf16 v[128:143], a[224:227], v[16:19], 0\n"
    "ds_read_b64_tr_b16 a[248:249], v10 offset:32768\n"
    "ds_read_b64_tr_b16 a[250:251], v10 offset:33792\n"
    "v_mfma_f32_32x32x16_bf16 v[128:143], a[228:231], v[20:23], v[128:143]\n"
    "ds_read_b64_tr_b16 a[252:253], v10 offset:34816\n"
    "ds_read_b64_tr_b16 a[254:255], v10 offset:35840\n"
    "v_mfma_f32_32x32x16_bf16 v[128:143], a[232:235], v[24:27], v[128:143]\n"
    "ds_read_b64_tr_b16 a[224:225], v10 offset:8192\n"
    "ds_read_b64_tr_b16 a[226:227], v10 offset:9216\n"
    "v_mul_f32 v212, v14, v144\n"
    "v_exp_f32 v212, v212\n"
    "v_mfma_f32_32x32x16_bf16 v[128:143], a[236:239], v[28:31], v[128:143]\n"
    "ds_read_b64_tr_b16 a[228:229], v10 offset:10240\n"
    "ds_read_b64_tr_b16 a[230:231], v10 offset:11264\n"
    "v_mul_f32 v213, v14, v145\n"
    "v_exp_f32 v213, v213\n"
    "s_waitcnt lgkmcnt(4)\n"
    "v_mfma_f32_32x32x16_bf16 v[128:143], a[240:243], v[32:35], v[128:143]\n"
    "ds_read_b64_tr_b16 a[232:233], v10 offset:36864\n"
    "ds_read_b64_tr_b16 a[234:235], v10 offset:37888\n"
    "v_mul_f32 v152, v144, v152\n"
    "v_fma_f32 v212, v212, v15, v15\n"
    "v_mfma_f32_32x32x16_bf16 v[128:143], a[244:247], v[36:39], v[128:143]\n"
    "ds_read_b64_tr_b16 a[236:237], v10 offset:38912\n"
    "ds_read_b64_tr_b16 a[238:239], v10 offset:39936\n"
    "v_rcp_f32 v212, v212\n"
    "v_fma_f32 v213, v213, v15, v15\n"
    "v_mfma_f32_32x32x16_bf16 v[128:143], a[248:251], v[40:43], v[128:143]\n"
    "ds_read_b64_tr_b16 a[240:241], v10 offset:12288\n"
    "ds_read_b64_tr_b16 a[242:243], v10 offset:13312\n"
    "v_rcp_f32 v213, v213\n"
    "v_mul_f32 v153, v145, v153\n"
    "v_mfma_f32_32x32x16_bf16 v[128:143], a[252:255], v[44:47], v[128:143]\n"
    "ds_read_b64_tr_b16 a[244:245], v10 offset:14336\n"
    "ds_read_b64_tr_b16 a[246:247], v10 offset:15360\n"
    "v_mul_f32 v152, v152, v212\n"
    "v_mul_f32 v153, v153, v213\n"
    "v_cvt_pk_bf16_f32 v144, v152, v153\n"
    "s_waitcnt lgkmcnt(4)\n"
    "v_mfma_f32_32x32x16_bf16 v[128:143], a[224:227], v[48:51], v[128:143]\n"
    "ds_read_b64_tr_b16 a[248:249], v10 offset:40960\n"
    "ds_read_b64_tr_b16 a[250:251], v10 offset:41984\n"
    "v_mul_f32 v214, v14, v146\n"
    "v_exp_f32 v214, v214\n"
    "v_mfma_f32_32x32x16_bf16 v[128:143], a[228:231], v[52:55], v[128:143]\n"
    "ds_read_b64_tr_b16 a[252:253], v10 offset:43008\n"
    "ds_read_b64_tr_b16 a[254:255], v10 offset:44032\n"
    "v_mul_f32 v215, v14, v147\n"
    "v_exp_f32 v215, v215\n"
    "v_mfma_f32_32x32x16_bf16 v[128:143], a[232:235], v[56:59], v[128:143]\n"
    "ds_read_b64_tr_b16 a[224:225], v10 offset:16384\n"
    "ds_read_b64_tr_b16 a[226:227], v10 offset:17408\n"
    "v_mul_f32 v154, v146, v154\n"
    "v_fma_f32 v214, v214, v15, v15\n"
    "v_mfma_f32_32x32x16_bf16 v[128:143], a[236:239], v[60:63], v[128:143]\n"
    "ds_read_b64_tr_b16 a[228:229], v10 offset:18432\n"
    "ds_read_b64_tr_b16 a[230:231], v10 offset:19456\n"
    "v_rcp_f32 v214, v214\n"
    "v_fma_f32 v215, v215, v15, v15\n"
    "s_waitcnt lgkmcnt(4)\n"
    "v_mfma_f32_32x32x16_bf16 v[128:143], a[240:243], v[64:67], v[128:143]\n"
    "ds_read_b64_tr_b16 a[232:233], v10 offset:45056\n"
    "ds_read_b64_tr_b16 a[234:235], v10 offset:46080\n"
    "v_rcp_f32 v215, v215\n"
    "v_mul_f32 v155, v147, v155\n"
    "v_mfma_f32_32x32x16_bf16 v[128:143], a[244:247], v[68:71], v[128:143]\n"
    "ds_read_b64_tr_b16 a[236:237], v10 offset:47104\n"
    "ds_read_b64_tr_b16 a[238:239], v10 offset:48128\n"
    "v_mul_f32 v154, v154, v214\n"
    "v_mul_f32 v155, v155, v215\n"
    "v_cvt_pk_bf16_f32 v145, v154, v155\n"
    "v_mfma_f32_32x32x16_bf16 v[128:143], a[248:251], v[72:75], v[128:143]\n"
    "ds_read_b64_tr_b16 a[240:241], v10 offset:20480\n"
    "ds_read_b64_tr_b16 a[242:243], v10 offset:21504\n"
    "v_mul_f32 v212, v14, v148\n"
    "v_exp_f32 v212, v212\n"
    "v_mfma_f32_32x32x16_bf16 v[128:143], a[252:255], v[76:79], v[128:143]\n"
    "ds_read_b64_tr_b16 a[244:245], v10 offset:22528\n"
    "ds_read_b64_tr_b16 a[246:247], v10 offset:23552\n"
    "v_mul_f32 v213, v14, v149\n"
    "v_exp_f32 v213, v213\n"
    "s_waitcnt lgkmcnt(4)\n"
    "v_mfma_f32_32x32x16_bf16 v[128:143], a[224:227], v[80:83], v[128:143]\n"
    "s_waitcnt vmcnt(7)\n"
    "s_barrier\n"
    "s_cmp_lg_u32 s44, 0\n"
    "s_cbranch_scc1 NP3_%=\n"
    "s_mov_b64 exec, 1\n"
    "buffer_store_dwordx2 v[222:223], off, s[32:35], s78 sc1\n"
    "s_mov_b64 exec, -1\n"
    "NP3_%=:\n"
    "ds_read_b64_tr_b16 a[248:249], v10 offset:49152\n"
    "ds_read_b64_tr_b16 a[250:251], v10 offset:50176\n"
    "v_mul_f32 v156, v148, v156\n"
    "v_fma_f32 v212, v212, v15, v15\n"
    "v_mfma_f32_32x32x16_bf16 v[128:143], a[228:231], v[84:87], v[128:143]\n"
    "ds_read_b64_tr_b16 a[252:253], v10 offset:51200\n"
    "ds_read_b64_tr_b16 a[254:255], v10 offset:52224\n"
    "v_rcp_f32 v212, v212\n"
    "v_fma_f32 v213, v213, v15, v15\n"
    "v_mfma_f32_32x32x16_bf16 v[128:143], a[232:235], v[88:91], v[128:143]\n"
    "ds_read_b64_tr_b16 a[224:225], v10 offset:24576\n"
    "ds_read_b64_tr_b16 a[226:227], v10 offset:25600\n"
    "v_rcp_f32 v213, v213\n"
    "v_mul_f32 v157, v149, v157\n"
    "v_mfma_f32_32x32x16_bf16 v[128:143], a[236:239], v[92:95], v[128:143]\n"
    "ds_read_b64_tr_b16 a[228:229], v10 offset:26624\n"
    "ds_read_b64_tr_b16 a[230:231], v10 offset:27648\n"
    "v_mul_f32 v156, v156, v212\n"
    "v_mul_f32 v157, v157, v213\n"
    "v_cvt_pk_bf16_f32 v146, v156, v157\n"
    "s_waitcnt lgkmcnt(4)\n"
    "v_mfma_f32_32x32x16_bf16 v[128:143], a[240:243], v[96:99], v[128:143]\n"
    "buffer_load_dwordx2 v[240:241], v216, s[32:35], 0 offen sc1\n"
    "ds_read_b64_tr_b16 a[232:233], v10 offset:53248\n"
    "ds_read_b64_tr_b16 a[234:235], v10 offset:54272\n"
    "v_mul_f32 v214, v14, v150\n"
    "v_exp_f32 v214, v214\n"
    "v_mfma_f32_32x32x16_bf16 v[128:143], a[244:247], v[100:103], v[128:143]\n"
    "ds_read_b64_tr_b16 a[236:237], v10 offset:55296\n"
    "ds_read_b64_tr_b16 a[238:239], v10 offset:56320\n"
    "v_mul_f32 v215, v14, v151\n"
    "v_exp_f32 v215, v215\n"
    "v_mfma_f32_32x32x16_bf16 v[128:143], a[248:251], v[104:107], v[128:143]\n"
    "ds_read_b64_tr_b16 a[240:241], v11 offset:0\n"
    "ds_read_b64_tr_b16 a[242:243], v11 offset:1024\n"
    "v_mul_f32 v158, v150, v158\n"
    "v_fma_f32 v214, v214, v15, v15\n"
    "v_mfma_f32_32x32x16_bf16 v[128:143], a[252:255], v[108:111], v[128:143]\n"
    "ds_read_b64_tr_b16 a[244:245], v11 offset:2048\n"
    "ds_read_b64_tr_b16 a[246:247], v11 offset:3072\n"
    "v_rcp_f32 v214, v214\n"
    "v_fma_f32 v215, v215, v15, v15\n"
    "s_waitcnt lgkmcnt(4)\n"
    "v_mfma_f32_32x32x16_bf16 v[128:143], a[224:227], v[112:115], v[128:143]\n"
    "ds_read_b64_tr_b16 a[248:249], v11 offset:28672\n"
    "ds_read_b64_tr_b16 a[250:251], v11 offset:29696\n"
    "v_rcp_f32 v215, v215\n"
    "v_mul_f32 v159, v151, v159\n"
    "v_mfma_f32_32x32x16_bf16 v[128:143], a[228:231], v[116:119], v[128:143]\n"
    "ds_read_b64_tr_b16 a[252:253], v11 offset:30720\n"
    "ds_read_b64_tr_b16 a[254:255], v11 offset:31744\n"
    "v_mul_f32 v158, v158, v214\n"
    "v_mul_f32 v159, v159, v215\n"
    "v_cvt_pk_bf16_f32 v147, v158, v159\n"
    "v_mfma_f32_32x32x16_bf16 v[128:143], a[232:235], v[120:123], v[128:143]\n"
    "ds_read_b64_tr_b16 a[224:225], v11 offset:4096\n"
    "ds_read_b64_tr_b16 a[226:227], v11 offset:5120\n"
    "s_add_u32 s51, s66, 0\n"
    "buffer_store_dwordx4 v[144:147], v1, s[32:35], s51 offen sc1\n"
    "v_mfma_f32_32x32x16_bf16 v[128:143], a[236:239], v[124:127], v[128:143]\n"
    "ds_read_b64_tr_b16 a[228:229], v11 offset:6144\n"
    "ds_read_b64_tr_b16 a[230:231], v11 offset:7168\n"
    "s_waitcnt lgkmcnt(4)\n"
    "v_mfma_f32_32x32x16_bf16 v[144:159], a[240:243], v[16:19], 0\n"
    "ds_read_b64_tr_b16 a[232:233], v11 offset:32768\n"
    "ds_read_b64_tr_b16 a[234:235], v11 offset:33792\n"
    "v_mfma_f32_32x32x16_bf16 v[144:159], a[244:247], v[20:23], v[144:159]\n"
    "ds_read_b64_tr_b16 a[236:237], v11 offset:34816\n"
    "ds_read_b64_tr_b16 a[238:239], v11 offset:35840\n"
    "v_mfma_f32_32x32x16_bf16 v[144:159], a[248:251], v[24:27], v[144:159]\n"
    "ds_read_b64_tr_b16 a[240:241], v11 offset:8192\n"
    "ds_read_b64_tr_b16 a[242:243], v11 offset:9216\n"
    "v_mul_f32 v212, v14, v128\n"
    "v_exp_f32 v212, v212\n"
    "v_mfma_f32_32x32x16_bf16 v[144:159], a[252:255], v[28:31], v[144:159]\n"
    "ds_read_b64_tr_b16 a[244:245], v11 offset:10240\n"
    "ds_read_b64_tr_b16 a[246:247], v11 offset:11264\n"
    "v_mul_f32 v213, v14, v129\n"
    "v_exp_f32 v213, v213\n"
    "s_waitcnt lgkmcnt(4)\n"
    "v_mfma_f32_32x32x16_bf16 v[144:159], a[224:227], v[32:35], v[144:159]\n"
    "ds_read_b64_tr_b16 a[248:249], v11 offset:36864\n"
    "ds_read_b64_tr_b16 a[250:251], v11 offset:37888\n"
    "v_mul_f32 v136, v128, v136\n"
    "v_fma_f32 v212, v212, v15, v15\n"
    "v_mfma_f32_32x32x16_bf16 v[144:159], a[228:231], v[36:39], v[144:159]\n"
    "ds_read_b64_tr_b16 a[252:253], v11 offset:38912\n"
    "ds_read_b64_tr_b16 a[254:255], v11 offset:39936\n"
    "v_rcp_f32 v212, v212\n"
    "v_fma_f32 v213, v213, v15, v15\n"
    "v_mfma_f32_32x32x16_bf16 v[144:159], a[232:235], v[40:43], v[144:159]\n"
    "ds_read_b64_tr_b16 a[224:225], v11 offset:12288\n"
    "ds_read_b64_tr_b16 a[226:227], v11 offset:13312\n"
    "v_rcp_f32 v213, v213\n"
    "v_mul_f32 v137, v129, v137\n"
    "v_mfma_f32_32x32x16_bf16 v[144:159], a[236:239], v[44:47], v[144:159]\n"
    "ds_read_b64_tr_b16 a[228:229], v11 offset:14336\n"
    "ds_read_b64_tr_b16 a[230:231], v11 offset:15360\n"
    "v_mul_f32 v136, v136, v212\n"
    "v_mul_f32 v137, v137, v213\n"
    "v_cvt_pk_bf16_f32 v128, v136, v137\n"
    "s_waitcnt lgkmcnt(4)\n"
    "v_mfma_f32_32x32x16_bf16 v[144:159], a[240:243], v[48:51], v[144:159]\n"
    "ds_read_b64_tr_b16 a[232:233], v11 offset:40960\n"
    "ds_read_b64_tr_b16 a[234:235], v11 offset:41984\n"
    "v_mul_f32 v214, v14, v130\n"
    "v_exp_f32 v214, v214\n"
    "v_mfma_f32_32x32x16_bf16 v[144:159], a[244:247], v[52:55], v[144:159]\n"
    "ds_read_b64_tr_b16 a[236:237], v11 offset:43008\n"
    "ds_read_b64_tr_b16 a[238:239], v11 offset:44032\n"
    "v_mul_f32 v215, v14, v131\n"
    "v_exp_f32 v215, v215\n"
    "v_mfma_f32_32x32x16_bf16 v[144:159], a[248:251], v[56:59], v[144:159]\n"
    "ds_read_b64_tr_b16 a[240:241], v11 offset:16384\n"
    "ds_read_b64_tr_b16 a[242:243], v11 offset:17408\n"
    "v_mul_f32 v138, v130, v138\n"
    "v_fma_f32 v214, v214, v15, v15\n"
    "v_mfma_f32_32x32x16_bf16 v[144:159], a[252:255], v[60:63], v[144:159]\n"
    "ds_read_b64_tr_b16 a[244:245], v11 offset:18432\n"
    "ds_read_b64_tr_b16 a[246:247], v11 offset:19456\n"
    "v_rcp_f32 v214, v214\n"
    "v_fma_f32 v215, v215, v15, v15\n"
    "s_waitcnt lgkmcnt(4)\n"
    "v_mfma_f32_32x32x16_bf16 v[144:159], a[224:227], v[64:67], v[144:159]\n"
    "ds_read_b64_tr_b16 a[248:249], v11 offset:45056\n"
    "ds_read_b64_tr_b16 a[250:251], v11 offset:46080\n"
    "v_rcp_f32 v215, v215\n"
    "v_mul_f32 v139, v131, v139\n"
    "v_mfma_f32_32x32x16_bf16 v[144:159], a[228:231], v[68:71], v[144:159]\n"
    "ds_read_b64_tr_b16 a[252:253], v11 offset:47104\n"
    "ds_read_b64_tr_b16 a[254:255], v11 offset:48128\n"
    "v_mul_f32 v138, v138, v214\n"
    "v_mul_f32 v139, v139, v215\n"
    "v_cvt_pk_bf16_f32 v129, v138, v139\n"
    "v_mfma_f32_32x32x16_bf16 v[144:159], a[232:235], v[72:75], v[144:159]\n"
    "ds_read_b64_tr_b16 a[224:225], v11 offset:20480\n"
    "ds_read_b64_tr_b16 a[226:227], v11 offset:21504\n"
    "v_mul_f32 v212, v14, v132\n"
    "v_exp_f32 v212, v212\n"
    "v_mfma_f32_32x32x16_bf16 v[144:159], a[236:239], v[76:79], v[144:159]\n"
    "ds_read_b64_tr_b16 a[228:229], v11 offset:22528\n"
    "ds_read_b64_tr_b16 a[230:231], v11 offset:23552\n"
    "v_mul_f32 v213, v14, v133\n"
    "v_exp_f32 v213, v213\n"
    "s_waitcnt lgkmcnt(4)\n"
    "v_mfma_f32_32x32x16_bf16 v[144:159], a[240:243], v[80:83], v[144:159]\n"
    "ds_read_b64_tr_b16 a[232:233], v11 offset:49152\n"
    "ds_read_b64_tr_b16 a[234:235], v11 offset:50176\n"
    "v_mul_f32 v140, v132, v140\n"
    "v_fma_f32 v212, v212, v15, v15\n"
    "v_mfma_f32_32x32x16_bf16 v[144:159], a[244:247], v[84:87], v[144:159]\n"
    "ds_read_b64_tr_b16 a[236:237], v11 offset:51200\n"
    "ds_read_b64_tr_b16 a[238:239], v11 offset:52224\n"
    "v_rcp_f32 v212, v212\n"
    "v_fma_f32 v213, v213, v15, v15\n"
    "v_mfma_f32_32x32x16_bf16 v[144:159], a[248:251], v[88:91], v[144:159]\n"
    "ds_read_b64_tr_b16 a[240:241], v11 offset:24576\n"
    "ds_read_b64_tr_b16 a[242:243], v11 offset:25600\n"
    "v_rcp_f32 v213, v213\n"
    "v_mul_f32 v141, v133, v141\n"
    "v_mfma_f32_32x32x16_bf16 v[144:159], a[252:255], v[92:95], v[144:159]\n"
    "ds_read_b64_tr_b16 a[244:245], v11 offset:26624\n"
    "ds_read_b64_tr_b16 a[246:247], v11 offset:27648\n"
    "v_mul_f32 v140, v140, v212\n"
    "v_mul_f32 v141, v141, v213\n"
    "v_cvt_pk_bf16_f32 v130, v140, v141\n"
    "s_waitcnt lgkmcnt(4)\n"
    "v_mfma_f32_32x32x16_bf16 v[144:159], a[224:227], v[96:99], v[144:159]\n"
    "s_waitcnt vmcnt(1)\n"
    "v_cmp_ne_u64 vcc, s[60:61], v[240:241]\n"
    "s_nop 1\n"
    "s_cmp_lg_u64 vcc, 0\n"
    "s_cbranch_scc1 SPIN4_%=\n"
    "BACK6_%=:\n"
    "ds_read_b64_tr_b16 a[248:249], v11 offset:53248\n"
    "ds_read_b64_tr_b16 a[250:251], v11 offset:54272\n"
    "v_mul_f32 v214, v14, v134\n"
    "v_exp_f32 v214, v214\n"
    "v_mfma_f32_32x32x16_bf16 v[144:159], a[228:231], v[100:103], v[144:159]\n"
    "ds_read_b64_tr_b16 a[252:253], v11 offset:55296\n"
    "ds_read_b64_tr_b16 a[254:255], v11 offset:56320\n"
    "buffer_load_dwordx4 a[0:3], v2, s[32:35], s67 offen sc1\n"
    "v_mul_f32 v215, v14, v135\n"
    "v_exp_f32 v215, v215\n"
    "v_mfma_f32_32x32x16_bf16 v[144:159], a[232:235], v[104:107], v[144:159]\n"
    "ds_read_b64_tr_b16 a[224:225], v12 offset:0\n"
    "ds_read_b64_tr_b16 a[226:227], v12 offset:1024\n"
    "v_mul_f32 v142, v134, v142\n"
    "v_fma_f32 v214, v214, v15, v15\n"
    "v_mfma_f32_32x32x16_bf16 v[144:159], a[236:239], v[108:111], v[144:159]\n"
    "ds_read_b64_tr_b16 a[228:229], v12 offset:2048\n"
    "ds_read_b64_tr_b16 a[230:231], v12 offset:3072\n"
    "buffer_load_dwordx4 a[4:7], v3, s[32:35], s67 offen sc1\n"
    "v_rcp_f32 v214, v214\n"
    "s_waitcnt lgkmcnt(4)\n"
    "v_mfma_f32_32x32x16_bf16 v[144:159], a[240:243], v[112:115], v[144:159]\n"
    "ds_read_b64_tr_b16 a[232:233], v12 offset:28672\n"
    "ds_read_b64_tr_b16 a[234:235], v12 offset:29696\n"
    "v_fma_f32 v215, v215, v15, v15\n"
    "v_rcp_f32 v215, v215\n"
    "v_mfma_f32_32x32x16_bf16 v[144:159], a[244:247], v[116:119], v[144:159]\n"
    "ds_read_b64_tr_b16 a[236:237], v12 offset:30720\n"
    "ds_read_b64_tr_b16 a[238:239], v12 offset:31744\n"
    "buffer_load_dwordx4 a[8:11], v2, s[32:35], s67 offen offset:2048 sc1\n"
    "v_mul_f32 v143, v135, v143\n"
    "v_mul_f32 v142, v142, v214\n"
    "v_mfma_f32_32x32x16_bf16 v[144:159], a[248:251], v[120:123], v[144:159]\n"
    "ds_read_b64_tr_b16 a[240:241], v12 offset:4096\n"
    "ds_read_b64_tr_b16 a[242:243], v12 offset:5120\n"
    "v_mul_f32 v143, v143, v215\n"
    "v_cvt_pk_bf16_f32 v131, v142, v143\n"
    "v_mfma_f32_32x32x16_bf16 v[144:159], a[252:255], v[124:127], v[144:159]\n"
    "ds_read_b64_tr_b16 a[244:245], v12 offset:6144\n"
    "ds_read_b64_tr_b16 a[246:247], v12 offset:7168\n"
    "buffer_load_dwordx4 a[12:15], v3, s[32:35], s67 offen offset:2048 sc1\n"
    "s_add_u32 s51, s66, 57344\n"
    "buffer_store_dwordx4 v[128:131], v1, s[32:35], s51 offen sc1\n"
    "s_waitcnt lgkmcnt(4)\n"
    "s_nop 0\n"
    "v_mfma_f32_32x32x16_bf16 v[128:143], a[224:227], v[16:19], 0\n"
    "ds_read_b64_tr_b16 a[248:249], v12 offset:32768\n"
    "ds_read_b64_tr_b16 a[250:251], v12 offset:33792\n"
    "v_mfma_f32_32x32x16_bf16 v[128:143], a[228:231], v[20:23], v[128:143]\n"
    "ds_read_b64_tr_b16 a[252:253], v12 offset:34816\n"
    "ds_read_b64_tr_b16 a[254:255], v12 offset:35840\n"
    "s_add_u32 s67, s67, 4096\n"
    "buffer_load_dwordx4 a[16:19], v2, s[32:35], s67 offen sc1\n"
    "v_mfma_f32_32x32x16_bf16 v[128:143], a[232:235], v[24:27], v[128:143]\n"
    "ds_read_b64_tr_b16 a[224:225], v12 offset:8192\n"
    "ds_read_b64_tr_b16 a[226:227], v12 offset:9216\n"
    "v_mul_f32 v212, v14, v144\n"
    "v_exp_f32 v212, v212\n"
    "v_mfma_f32_32x32x16_bf16 v[128:143], a[236:239], v[28:31], v[128:143]\n"
    "ds_read_b64_tr_b16 a[228:229], v12 offset:10240\n"
    "ds_read_b64_tr_b16 a[230:231], v12 offset:11264\n"
    "buffer_load_dwordx4 a[20:23], v3, s[32:35], s67 offen sc1\n"
    "v_mul_f32 v213, v14, v145\n"
    "v_exp_f32 v213, v213\n"
    "s_waitcnt lgkmcnt(4)\n"
    "v_mfma_f32_32x32x16_bf16 v[128:143], a[240:243], v[32:35], v[128:143]\n"
    "ds_read_b64_tr_b16 a[232:233], v12 offset:36864\n"
    "ds_read_b64_tr_b16 a[234:235], v12 offset:37888\n"
    "v_mul_f32 v152, v144, v152\n"
    "v_fma_f32 v212, v212, v15, v15\n"
    "v_mfma_f32_32x32x16_bf16 v[128:143], a[244:247], v[36:39], v[128:143]\n"
    "ds_read_b64_tr_b16 a[236:237], v12 offset:38912\n"
    "ds_read_b64_tr_b16 a[238:239], v12 offset:39936\n"
    "buffer_load_dwordx4 a[24:27], v2, s[32:35], s67 offen offset:2048 sc1\n"
    "v_rcp_f32 v212, v212\n"
    "v_fma_f32 v213, v213, v15, v15\n"
    "v_mfma_f32_32x32x16_bf16 v[128:143], a[248:251], v[40:43], v[128:143]\n"
    "ds_read_b64_tr_b16 a[240:241], v12 offset:12288\n"
    "ds_read_b64_tr_b16 a[242:243], v12 offset:13312\n"
    "v_rcp_f32 v213, v213\n"
    "v_mul_f32 v153, v145, v153\n"
    "v_mfma_f32_32x32x16_bf16 v[128:143], a[252:255], v[44:47], v[128:143]\n"
    "s_barrier\n"
    "ds_read_b64_tr_b16 a[244:245], v12 offset:14336\n"
    "ds_read_b64_tr_b16 a[246:247], v12 offset:15360\n"
    "s_add_u32 m0, s45, 0\n"
    "s_add_u32 s23, s7, 128\n"
    "buffer_load_dwordx4 v4, s[28:31], s23 offen lds\n"
    "v_mul_f32 v152, v152, v212\n"
    "v_mul_f32 v153, v153, v213\n"
    "v_cvt_pk_bf16_f32 v144, v152, v153\n"
    "s_waitcnt lgkmcnt(4)\n"
    "v_mfma_f32_32x32x16_bf16 v[128:143], a[224:227], v[48:51], v[128:143]\n"
    "ds_read_b64_tr_b16 a[248:249], v12 offset:40960\n"
    "ds_read_b64_tr_b16 a[250:251], v12 offset:41984\n"
    "v_mul_f32 v214, v14, v146\n"
    "v_exp_f32 v214, v214\n"
    "v_mfma_f32_32x32x16_bf16 v[128:143], a[228:231], v[52:55], v[128:143]\n"
    "ds_read_b64_tr_b16 a[252:253], v12 offset:43008\n"
    "ds_read_b64_tr_b16 a[254:255], v12 offset:44032\n"
    "buffer_load_dwordx4 v252, s[28:31], s23 offen offset:1024 lds\n"
    "v_mul_f32 v215, v14, v147\n"
    "v_exp_f32 v215, v215\n"
    "v_mfma_f32_32x32x16_bf16 v[128:143], a[232:235], v[56:59], v[128:143]\n"
    "ds_read_b64_tr_b16 a[224:225], v12 offset:16384\n"
    "ds_read_b64_tr_b16 a[226:227], v12 offset:17408\n"
    "v_mul_f32 v154, v146, v154\n"
    "v_fma_f32 v214, v214, v15, v15\n"
    "v_mfma_f32_32x32x16_bf16 v[128:143], a[236:239], v[60:63], v[128:143]\n"
    "ds_read_b64_tr_b16 a[228:229], v12 offset:18432\n"
    "ds_read_b64_tr_b16 a[230:231], v12 offset:19456\n"
    "buffer_load_dwordx4 v253, s[28:31], s23 offen offset:2048 lds\n"
    "v_rcp_f32 v214, v214\n"
    "v_fma_f32 v215, v215, v15, v15\n"
    "s_waitcnt lgkmcnt(4)\n"
    "v_mfma_f32_32x32x16_bf16 v[128:143], a[240:243], v[64:67], v[128:143]\n"
    "ds_read_b64_tr_b16 a[232:233], v12 offset:45056\n"
    "ds_read_b64_tr_b16 a[234:235], v12 offset:46080\n"
    "v_rcp_f32 v215, v215\n"
    "v_mul_f32 v155, v147, v155\n"
    "v_mfma_f32_32x32x16_bf16 v[128:143], a[244:247], v[68:71], v[128:143]\n"
    "ds_read_b64_tr_b16 a[236:237], v12 offset:47104\n"
    "ds_read_b64_tr_b16 a[238:239], v12 offset:48128\n"
    "buffer_load_dwordx4 v254, s[28:31], s23 offen offset:3072 lds\n"
    "v_mul_f32 v154, v154, v214\n"
    "v_mul_f32 v155, v155, v215\n"
    "v_cvt_pk_bf16_f32 v145, v154, v155\n"
    "v_mfma_f32_32x32x16_bf16 v[128:143], a[248:251], v[72:75], v[128:143]\n"
    "ds_read_b64_tr_b16 a[240:241], v12 offset:20480\n"
    "ds_read_b64_tr_b16 a[242:243], v12 offset:21504\n"
    "v_mul_f32 v212, v14, v148\n"
    "v_exp_f32 v212, v212\n"
    "v_mfma_f32_32x32x16_bf16 v[128:143], a[252:255], v[76:79], v[128:143]\n"
    "s_barrier\n"
    "ds_read_b64_tr_b16 a[244:245], v12 offset:22528\n"
    "ds_read_b64_tr_b16 a[246:247], v12 offset:23552\n"
    "s_add_u32 m0, s45, 4096\n"
    "s_add_u32 s23, s8, 128\n"
    "buffer_load_dwordx4 v4, s[28:31], s23 offen lds\n"
    "v_mul_f32 v213, v14, v149\n"
    "v_exp_f32 v213, v213\n"
    "s_waitcnt lgkmcnt(4)\n"
    "v_mfma_f32_32x32x16_bf16 v[128:143], a[224:227], v[80:83], v[128:143]\n"
    "ds_read_b64_tr_b16 a[248:249], v12 offset:49152\n"
    "ds_read_b64_tr_b16 a[250:251], v12 offset:50176\n"
    "v_mul_f32 v156, v148, v156\n"
    "v_fma_f32 v212, v212, v15, v15\n"
    "v_mfma_f32_32x32x16_bf16 v[128:143], a[228:231], v[84:87], v[128:143]\n"
    "ds_read_b64_tr_b16 a[252:253], v12 offset:51200\n"
    "ds_read_b64_tr_b16 a[254:255], v12 offset:52224\n"
    "buffer_load_dwordx4 v252, s[28:31], s23 offen offset:1024 lds\n"
    "v_rcp_f32 v212, v212\n"
    "v_fma_f32 v213, v213, v15, v15\n"
    "v_mfma_f32_32x32x16_bf16 v[128:143], a[232:235], v[88:91], v[128:143]\n"
    "ds_read_b64_tr_b16 a[224:225], v12 offset:24576\n"
    "ds_read_b64_tr_b16 a[226:227], v12 offset:25600\n"
    "v_rcp_f32 v213, v213\n"
    "v_mul_f32 v157, v149, v157\n"
    "v_mfma_f32_32x32x16_bf16 v[128:143], a[236:239], v[92:95], v[128:143]\n"
    "ds_read_b64_tr_b16 a[228:229], v12 offset:26624\n"
    "ds_read_b64_tr_b16 a[230:231], v12 offset:27648\n"
    "buffer_load_dwordx4 v253, s[28:31], s23 offen offset:2048 lds\n"
    "v_mul_f32 v156, v156, v212\n"
    "v_mul_f32 v157, v157, v213\n"
    "s_waitcnt lgkmcnt(4)\n"
    "v_mfma_f32_32x32x16_bf16 v[128:143], a[240:243], v[96:99], v[128:143]\n"
    "ds_read_b64_tr_b16 a[232:233], v12 offset:53248\n"
    "ds_read_b64_tr_b16 a[234:235], v12 offset:54272\n"
    "v_cvt_pk_bf16_f32 v146, v156, v157\n"
    "v_mul_f32 v214, v14, v150\n"
    "v_mfma_f32_32x32x16_bf16 v[128:143], a[244:247], v[100:103], v[128:143]\n"
    "ds_read_b64_tr_b16 a[236:237], v12 offset:55296\n"
    "ds_read_b64_tr_b16 a[238:239], v12 offset:56320\n"
    "buffer_load_dwordx4 v254, s[28:31], s23 offen offset:3072 lds\n"
    "v_exp_f32 v214, v214\n"
    "v_mul_f32 v215, v14, v151\n"
    "v_mfma_f32_32x32x16_bf16 v[128:143], a[248:251], v[104:107], v[128:143]\n"
    "s_barrier\n"
    "ds_read_b64_tr_b16 a[240:241], v5 offset:0\n"
    "ds_read_b64_tr_b16 a[242:243], v5 offset:1024\n"
    "v_exp_f32 v215, v215\n"
    "v_mul_f32 v158, v150, v158\n"
    "v_mfma_f32_32x32x16_bf16 v[128:143], a[252:255], v[108:111], v[128:143]\n"
    "ds_read_b64_tr_b16 a[244:245], v6 offset:0\n"
    "ds_read_b64_tr_b16 a[246:247], v6 offset:1024\n"
    "s_add_u32 m0, s45, 8192\n"
    "s_add_u32 s23, s9, 128\n"
    "buffer_load_dwordx4 v4, s[28:31], s23 offen lds\n"
    "v_fma_f32 v214, v214, v15, v15\n"
    "v_rcp_f32 v214, v214\n"
    "s_waitcnt lgkmcnt(4)\n"
    "v_mfma_f32_32x32x16_bf16 v[128:143], a[224:227], v[112:115], v[128:143]\n"
    "ds_read_b64_tr_b16 a[248:249], v7 offset:0\n"
    "ds_read_b64_tr_b16 a[250:251], v7 offset:1024\n"
    "v_fma_f32 v215, v215, v15, v15\n"
    "v_rcp_f32 v215, v215\n"
    "v_mfma_f32_32x32x16_bf16 v[128:143], a[228:231], v[116:119], v[128:143]\n"
    "ds_read_b64_tr_b16 a[252:253], v8 offset:0\n"
    "ds_read_b64_tr_b16 a[254:255], v8 offset:1024\n"
    "buffer_load_dwordx4 v252, s[28:31], s23 offen offset:1024 lds\n"
    "v_mul_f32 v159, v151, v159\n"
    "v_mul_f32 v158, v158, v214\n"
    "v_mfma_f32_32x32x16_bf16 v[128:143], a[232:235], v[120:123], v[128:143]\n"
    "ds_read_b64_tr_b16 a[224:225], v5 offset:2048\n"
    "ds_read_b64_tr_b16 a[226:227], v5 offset:3072\n"
    "v_mul_f32 v159, v159, v215\n"
    "v_cvt_pk_bf16_f32 v147, v158, v159\n"
    "v_mfma_f32_32x32x16_bf16 v[128:143], a[236:239], v[124:127], v[128:143]\n"
    "ds_read_b64_tr_b16 a[228:229], v6 offset:2048\n"
    "ds_read_b64_tr_b16 a[230:231], v6 offset:3072\n"
    "buffer_load_dwordx4 v253, s[28:31], s23 offen offset:2048 lds\n"
    "s_add_u32 s51, s66, 114688\n"
    "buffer_store_dwordx4 v[144:147], v1, s[32:35], s51 offen sc1\n"
    "s_waitcnt lgkmcnt(4)\n"
    "s_nop 0\n"
    "v_mfma_f32_32x32x16_bf16 v[144:159], a[240:243], v[16:19], 0\n"
    "ds_read_b64_tr_b16 a[232:233], v7 offset:2048\n"
    "ds_read_b64_tr_b16 a[234:235], v7 offset:3072\n"
    "v_mfma_f32_32x32x16_bf16 v[160:175], a[244:247], v[16:19], 0\n"
    "ds_read_b64_tr_b16 a[236:237], v8 offset:2048\n"
    "ds_read_b64_tr_b16 a[238:239], v8 offset:3072\n"
    "buffer_load_dwordx4 v254, s[28:31], s23 offen offset:3072 lds\n"
    "v_mfma_f32_32x32x16_bf16 v[176:191], a[248:251], v[16:19], 0\n"
    "ds_read_b64_tr_b16 a[240:241], v5 offset:4096\n"
    "ds_read_b64_tr_b16 a[242:243], v5 offset:5120\n"
    "v_mul_f32 v212, v14, v128\n"
    "v_exp_f32 v212, v212\n"
    "v_mfma_f32_32x32x16_bf16 v[192:207], a[252:255], v[16:19], 0\n"
    "ds_read_b64_tr_b16 a[244:245], v6 offset:4096\n"
    "ds_read_b64_tr_b16 a[246:247], v6 offset:5120\n"
    "s_add_u32 m0, s45, 12288\n"
    "s_add_u32 s23, s10, 128\n"
    "buffer_load_dwordx4 v4, s[28:31], s23 offen lds\n"
    "v_mul_f32 v213, v14, v129\n"
    "v_exp_f32 v213, v213\n"
    "s_waitcnt lgkmcnt(4)\n"
    "v_mfma_f32_32x32x16_bf16 v[144:159], a[224:227], v[20:23], v[144:159]\n"
    "ds_read_b64_tr_b16 a[248:249], v7 offset:4096\n"
    "ds_read_b64_tr_b16 a[250:251], v7 offset:5120\n"
    "v_mul_f32 v136, v128, v136\n"
    "v_fma_f32 v212, v212, v15, v15\n"
    "v_mfma_f32_32x32x16_bf16 v[160:175], a[228:231], v[20:23], v[160:175]\n"
    "ds_read_b64_tr_b16 a[252:253], v8 offset:4096\n"
    "ds_read_b64_tr_b16 a[254:255], v8 offset:5120\n"
    "buffer_load_dwordx4 v252, s[28:31], s23 offen offset:1024 lds\n"
    "v_rcp_f32 v212, v212\n"
    "v_fma_f32 v213, v213, v15, v15\n"
    "v_mfma_f32_32x32x16_bf16 v[176:191], a[232:235], v[20:23], v[176:191]\n"
    "ds_read_b64_tr_b16 a[224:225], v5 offset:6144\n"
    "ds_read_b64_tr_b16 a[226:227], v5 offset:7168\n"
    "v_rcp_f32 v213, v213\n"
    "v_mul_f32 v137, v129, v137\n"
    "v_mfma_f32_32x32x16_bf16 v[192:207], a[236:239], v[20:23], v[192:207]\n"
    "ds_read_b64_tr_b16 a[228:229], v6 offset:6144\n"
    "ds_read_b64_tr_b16 a[230:231], v6 offset:7168\n"
    "buffer_load_dwordx4 v253, s[28:31], s23 offen offset:2048 lds\n"
    "v_mul_f32 v136, v136, v212\n"
    "v_mul_f32 v137, v137, v213\n"
    "v_cvt_pk_bf16_f32 v128, v136, v137\n"
    "s_waitcnt lgkmcnt(4)\n"
    "v_mfma_f32_32x32x16_bf16 v[144:159], a[240:243], v[24:27], v[144:159]\n"
    "ds_read_b64_tr_b16 a[232:233], v7 offset:6144\n"
    "ds_read_b64_tr_b16 a[234:235], v7 offset:7168\n"
    "v_mul_f32 v214, v14, v130\n"
    "v_exp_f32 v214, v214\n"
    "v_mfma_f32_32x32x16_bf16 v[160:175], a[244:247], v[24:27], v[160:175]\n"
    "ds_read_b64_tr_b16 a[236:237], v8 offset:6144\n"
    "ds_read_b64_tr_b16 a[238:239], v8 offset:7168\n"
    "buffer_load_dwordx4 v254, s[28:31], s23 offen offset:3072 lds\n"
    "v_mul_f32 v215, v14, v131\n"
    "v_exp_f32 v215, v215\n"
    "v_mfma_f32_32x32x16_bf16 v[176:191], a[248:251], v[24:27], v[176:191]\n"
    "ds_read_b64_tr_b16 a[240:241], v5 offset:16384\n"
    "ds_read_b64_tr_b16 a[242:243], v5 offset:17408\n"
    "v_mul_f32 v138, v130, v138\n"
    "v_fma_f32 v214, v214, v15, v15\n"
    "v_mfma_f32_32x32x16_bf16 v[192:207], a[252:255], v[24:27], v[192:207]\n"
    "ds_read_b64_tr_b16 a[244:245], v6 offset:16384\n"
    "ds_read_b64_tr_b16 a[246:247], v6 offset:17408\n"
    "buffer_load_dwordx4 a[28:31], v3, s[32:35], s67 offen offset:2048 sc1\n"
    "v_rcp_f32 v214, v214\n"
    "v_fma_f32 v215, v215, v15, v15\n"
    "s_waitcnt lgkmcnt(4)\n"
    "v_mfma_f32_32x32x16_bf16 v[144:159], a[224:227], v[28:31], v[144:159]\n"
    "ds_read_b64_tr_b16 a[248:249], v7 offset:16384\n"
    "ds_read_b64_tr_b16 a[250:251], v7 offset:17408\n"
    "v_rcp_f32 v215, v215\n"
    "v_mul_f32 v139, v131, v139\n"
    "v_mfma_f32_32x32x16_bf16 v[160:175], a[228:231], v[28:31], v[160:175]\n"
    "ds_read_b64_tr_b16 a[252:253], v8 offset:16384\n"
    "ds_read_b64_tr_b16 a[254:255], v8 offset:17408\n"
    "s_add_u32 s67, s67, 4096\n"
    "buffer_load_dwordx4 a[32:35], v2, s[32:35], s67 offen sc1\n"
    "v_mul_f32 v138, v138, v214\n"
    "v_mul_f32 v139, v139, v215\n"
    "v_cvt_pk_bf16_f32 v129, v138, v139\n"
    "v_mfma_f32_32x32x16_bf16 v[176:191], a[232:235], v[28:31], v[176:191]\n"
    "ds_read_b64_tr_b16 a[224:225], v5 offset:18432\n"
    "ds_read_b64_tr_b16 a[226:227], v5 offset:19456\n"
    "v_mul_f32 v212, v14, v132\n"
    "v_exp_f32 v212, v212\n"
    "v_mfma_f32_32x32x16_bf16 v[192:207], a[236:239], v[28:31], v[192:207]\n"
    "ds_read_b64_tr_b16 a[228:229], v6 offset:18432\n"
    "ds_read_b64_tr_b16 a[230:231], v6 offset:19456\n"
    "buffer_load_dwordx4 a[36:39], v3, s[32:35], s67 offen sc1\n"
    "v_mul_f32 v213, v14, v133\n"
    "v_exp_f32 v213, v213\n"
    "s_waitcnt lgkmcnt(4)\n"
    "v_mfma_f32_32x32x16_bf16 v[144:159], a[240:243], v[32:35], v[144:159]\n"
    "ds_read_b64_tr_b16 a[232:233], v7 offset:18432\n"
    "ds_read_b64_tr_b16 a[234:235], v7 offset:19456\n"
    "v_mul_f32 v140, v132, v140\n"
    "v_fma_f32 v212, v212, v15, v15\n"
    "v_mfma_f32_32x32x16_bf16 v[160:175], a[244:247], v[32:35], v[160:175]\n"
    "ds_read_b64_tr_b16 a[236:237], v8 offset:18432\n"
    "ds_read_b64_tr_b16 a[238:239], v8 offset:19456\n"
    "buffer_load_dwordx4 a[40:43], v2, s[32:35], s67 offen offset:2048 sc1\n"
    "v_rcp_f32 v212, v212\n"
    "v_fma_f32 v213, v213, v15, v15\n"
    "v_mfma_f32_32x32x16_bf16 v[176:191], a[248:251], v[32:35], v[176:191]\n"
    "ds_read_b64_tr_b16 a[240:241], v5 offset:20480\n"
    "ds_read_b64_tr_b16 a[242:243], v5 offset:21504\n"
    "v_rcp_f32 v213, v213\n"
    "v_mul_f32 v141, v133, v141\n"
    "v_mfma_f32_32x32x16_bf16 v[192:207], a[252:255], v[32:35], v[192:207]\n"
    "ds_read_b64_tr_b16 a[244:245], v6 offset:20480\n"
    "ds_read_b64_tr_b16 a[246:247], v6 offset:21504\n"
    "buffer_load_dwordx4 a[44:47], v3, s[32:35], s67 offen offset:2048 sc1\n"
    "v_mul_f32 v140, v140, v212\n"
    "v_mul_f32 v141, v141, v213\n"
    "v_cvt_pk_bf16_f32 v130, v140, v141\n"
    "s_waitcnt lgkmcnt(4)\n"
    "v_mfma_f32_32x32x16_bf16 v[144:159], a[224:227], v[36:39], v[144:159]\n"
    "ds_read_b64_tr_b16 a[248:249], v7 offset:20480\n"
    "ds_read_b64_tr_b16 a[250:251], v7 offset:21504\n"
    "v_mul_f32 v214, v14, v134\n"
    "v_exp_f32 v214, v214\n"
    "v_mfma_f32_32x32x16_bf16 v[160:175], a[228:231], v[36:39], v[160:175]\n"
    "ds_read_b64_tr_b16 a[252:253], v8 offset:20480\n"
    "ds_read_b64_tr_b16 a[254:255], v8 offset:21504\n"
    "s_add_u32 s67, s67, 4096\n"
    "buffer_load_dwordx4 a[48:51], v2, s[32:35], s67 offen sc1\n"
    "v_mul_f32 v215, v14, v135\n"
    "v_exp_f32 v215, v215\n"
    "v_mfma_f32_32x32x16_bf16 v[176:191], a[232:235], v[36:39], v[176:191]\n"
    "ds_read_b64_tr_b16 a[224:225], v5 offset:22528\n"
    "ds_read_b64_tr_b16 a[226:227], v5 offset:23552\n"
    "v_mul_f32 v142, v134, v142\n"
    "v_fma_f32 v214, v214, v15, v15\n"
    "v_rcp_f32 v214, v214\n"
    "v_mfma_f32_32x32x16_bf16 v[192:207], a[236:239], v[36:39], v[192:207]\n"
    "ds_read_b64_tr_b16 a[228:229], v6 offset:22528\n"
    "ds_read_b64_tr_b16 a[230:231], v6 offset:23552\n"
    "buffer_load_dwordx4 a[52:55], v3, s[32:35], s67 offen sc1\n"
    "v_fma_f32 v215, v215, v15, v15\n"
    "v_rcp_f32 v215, v215\n"
    "s_waitcnt lgkmcnt(4)\n"
    "v_mfma_f32_32x32x16_bf16 v[144:159], a[240:243], v[40:43], v[144:159]\n"
    "ds_read_b64_tr_b16 a[232:233], v7 offset:22528\n"
    "ds_read_b64_tr_b16 a[234:235], v7 offset:23552\n"
    "v_mul_f32 v143, v135, v143\n"
    "v_mul_f32 v142, v142, v214\n"
    "v_mul_f32 v143, v143, v215\n"
    "v_mfma_f32_32x32x16_bf16 v[160:175], a[244:247], v[40:43], v[160:175]\n"
    "ds_read_b64_tr_b16 a[236:237], v8 offset:22528\n"
    "ds_read_b64_tr_b16 a[238:239], v8 offset:23552\n"
    "buffer_load_dwordx4 a[56:59], v2, s[32:35], s67 offen offset:2048 sc1\n"
    "v_cvt_pk_bf16_f32 v131, v142, v143\n"
    "s_add_u32 s51, s66, 172032\n"
    "buffer_store_dwordx4 v[128:131], v1, s[32:35], s51 offen sc1\n"
    "v_mfma_f32_32x32x16_bf16 v[176:191], a[248:251], v[40:43], v[176:191]\n"
    "s_cmp_lg_u32 s44, 0\n"
    "s_cbranch_scc1 NQ7_%=\n"
    "buffer_load_dwordx2 v[240:241], v217, s[32:35], 0 offen sc1\n"
    "NQ7_%=:\n"
    "ds_read_b64_tr_b16 a[240:241], v5 offset:32768\n"
    "ds_read_b64_tr_b16 a[242:243], v5 offset:33792\n"
    "v_and_b32 v136, 15, v0\n"
    "v_lshl_add_u32 v136, s44, 4, v136\n"
    "v_cvt_f32_u32 v136, v136\n"
    "v_mfma_f32_32x32x16_bf16 v[192:207], a[252:255], v[40:43], v[192:207]\n"
    "ds_read_b64_tr_b16 a[244:245], v5 offset:34816\n"
    "ds_read_b64_tr_b16 a[246:247], v5 offset:35840\n"
    "buffer_load_dwordx4 a[60:63], v3, s[32:35], s67 offen offset:2048 sc1\n"
    "v_mul_f32 v136, 0x3a000000, v136\n"
    "v_lshrrev_b32 v137, 4, v0\n"
    "v_lshlrev_b32 v137, 2, v137\n"
    "s_waitcnt lgkmcnt(4)\n"
    "v_mfma_f32_32x32x16_bf16 v[144:159], a[224:227], v[44:47], v[144:159]\n"
    "ds_read_b64_tr_b16 a[248:249], v5 offset:36864\n"
    "ds_read_b64_tr_b16 a[250:251], v5 offset:37888\n"
    "v_cvt_f32_u32 v137, v137\n"
    "v_mul_f32 v137, v136, v137\n"
    "v_fmamk_f32 v232, v136, 0x0, v137\n"
    "v_mfma_f32_32x32x16_bf16 v[160:175], a[228:231], v[44:47], v[160:175]\n"
    "ds_read_b64_tr_b16 a[252:253], v5 offset:38912\n"
    "ds_read_b64_tr_b16 a[254:255], v5 offset:39936\n"
    "s_add_u32 s67, s67, 4096\n"
    "buffer_load_dwordx4 a[64:67], v2, s[32:35], s67 offen sc1\n"
    "v_cos_f32 v224, v232\n"
    "v_sin_f32 v232, v232\n"
    "v_mfma_f32_32x32x16_bf16 v[176:191], a[232:235], v[44:47], v[176:191]\n"
    "s_waitcnt vmcnt(20)\n"
    "s_barrier\n"
    "ds_read_b64_tr_b16 a[224:225], v9 offset:0\n"
    "ds_read_b64_tr_b16 a[226:227], v9 offset:1024\n"
    "v_fmamk_f32 v233, v136, 0x3f800000, v137\n"
    "v_cos_f32 v225, v233\n"
    "v_mfma_f32_32x32x16_bf16 v[192:207], a[236:239], v[44:47], v[192:207]\n"
    "ds_read_b64_tr_b16 a[228:229], v9 offset:2048\n"
    "ds_read_b64_tr_b16 a[230:231], v9 offset:3072\n"
    "buffer_load_dwordx4 a[68:71], v3, s[32:35], s67 offen sc1\n"
    "v_sin_f32 v233, v233\n"
    "v_fmamk_f32 v234, v136, 0x40000000, v137\n"
    "s_waitcnt lgkmcnt(4)\n"
    "v_mfma_f32_32x32x16_bf16 v[144:159], a[240:243], v[48:51], v[144:159]\n"
    "ds_read_b64_tr_b16 a[232:233], v9 offset:28672\n"
    "ds_read_b64_tr_b16 a[234:235], v9 offset:29696\n"
    "v_cos_f32 v226, v234\n"
    "v_mfma_f32_32x32x16_bf16 v[144:159], a[244:247], v[52:55], v[144:159]\n"
    "ds_read_b64_tr_b16 a[236:237], v9 offset:30720\n"
    "ds_read_b64_tr_b16 a[238:239], v9 offset:31744\n"
    "buffer_load_dwordx4 a[72:75], v2, s[32:35], s67 offen offset:2048 sc1\n"
    "v_sin_f32 v234, v234\n"
    "v_fmamk_f32 v235, v136, 0x40400000, v137\n"
    "v_mfma_f32_32x32x16_bf16 v[144:159], a[248:251], v[56:59], v[144:159]\n"
    "ds_read_b64_tr_b16 a[240:241], v9 offset:4096\n"
    "ds_read_b64_tr_b16 a[242:243], v9 offset:5120\n"
    "v_cos_f32 v227, v235\n"
    "v_sin_f32 v235, v235\n"
    "v_mfma_f32_32x32x16_bf16 v[144:159], a[252:255], v[60:63], v[144:159]\n"
    "ds_read_b64_tr_b16 a[244:245], v9 offset:6144\n"
    "ds_read_b64_tr_b16 a[246:247], v9 offset:7168\n"
    "buffer_load_dwordx4 a[76:79], v3, s[32:35], s67 offen offset:2048 sc1\n"
    "v_fmamk_f32 v236, v136, 0x41800000, v137\n"
    "v_cos_f32 v228, v236\n"
    "s_waitcnt lgkmcnt(4)\n"
    "v_mfma_f32_32x32x16_bf16 v[144:159], a[224:227], v[64:67], v[144:159]\n"
    "ds_read_b64_tr_b16 a[248:249], v9 offset:32768\n"
    "ds_read_b64_tr_b16 a[250:251], v9 offset:33792\n"
    "v_sin_f32 v236, v236\n"
    "v_fmamk_f32 v237, v136, 0x41880000, v137\n"
    "v_mfma_f32_32x32x16_bf16 v[144:159], a[228:231], v[68:71], v[144:159]\n"
    "ds_read_b64_tr_b16 a[252:253], v9 offset:34816\n"
    "ds_read_b64_tr_b16 a[254:255], v9 offset:35840\n"
    "s_add_u32 s67, s67, 4096\n"
    "buffer_load_dwordx4 a[80:83], v2, s[32:35], s67 offen sc1\n"
    "v_cos_f32 v229, v237\n"
    "v_mfma_f32_32x32x16_bf16 v[144:159], a[232:235], v[72:75], v[144:159]\n"
    "s_waitcnt vmcnt(15)\n"
    "s_barrier\n"
    "ds_read_b64_tr_b16 a[224:225], v9 offset:8192\n"
    "ds_read_b64_tr_b16 a[226:227], v9 offset:9216\n"
    "v_sin_f32 v237, v237\n"
    "v_fmamk_f32 v238, v136, 0x41900000, v137\n"
    "v_mfma_f32_32x32x16_bf16 v[144:159], a[236:239], v[76:79], v[144:159]\n"
    "ds_read_b64_tr_b16 a[228:229], v9 offset:10240\n"
    "ds_read_b64_tr_b16 a[230:231], v9 offset:11264\n"
    "s_mul_i32 s71, s44, 5120\n"
    "s_add_u32 m0, s71, 114688\n"
    "s_add_u32 s51, s71, 602112\n"
    "buffer_load_dwordx4 v1, s[36:39], s51 offen lds\n"
    "v_cos_f32 v230, v238\n"
    "v_sin_f32 v238, v238\n"
    "s_waitcnt lgkmcnt(4)\n"
    "v_mfma_f32_32x32x16_bf16 v[144:159], a[240:243], v[80:83], v[144:159]\n"
    "ds_read_b64_tr_b16 a[232:233], v9 offset:36864\n"
    "ds_read_b64_tr_b16 a[234:235], v9 offset:37888\n"
    "v_fmamk_f32 v239, v136, 0x41980000, v137\n"
    "v_cos_f32 v231, v239\n"
    "v_mfma_f32_32x32x16_bf16 v[144:159], a[244:247], v[84:87], v[144:159]\n"
    "ds_read_b64_tr_b16 a[236:237], v9 offset:38912\n"
    "ds_read_b64_tr_b16 a[238:239], v9 offset:39936\n"
    "s_add_u32 m0, s71, 115712\n"
    "s_add_u32 s51, s71, 603136\n"
    "buffer_load_dwordx4 v1, s[36:39], s51 offen lds\n"
    "v_sin_f32 v239, v239\n"
    "v_and_b32 v136, 15, v0\n"
    "v_mfma_f32_32x32x16_bf16 v[144:159], a[248:251], v[88:91], v[144:159]\n"
    "ds_read_b64_tr_b16 a[240:241], v9 offset:12288\n"
    "ds_read_b64_tr_b16 a[242:243], v9 offset:13312\n"
    "v_lshrrev_b32 v137, 4, v0\n"
    "v_lshrrev_b32 v242, 1, v136\n"
    "v_xor_b32 v242, v242, v137\n"
    "v_mfma_f32_32x32x16_bf16 v[144:159], a[252:255], v[92:95], v[144:159]\n"
    "ds_read_b64_tr_b16 a[244:245], v9 offset:14336\n"
    "ds_read_b64_tr_b16 a[246:247], v9 offset:15360\n"
    "s_add_u32 m0, s71, 116736\n"
    "s_add_u32 s51, s71, 604160\n"
    "buffer_load_dwordx4 v1, s[36:39], s51 offen lds\n"
    "v_lshlrev_b32 v242, 4, v242\n"
    "v_lshl_add_u32 v242, v136, 7, v242\n"
    "v_add_u32 v242, 135168, v242\n"
    "s_waitcnt lgkmcnt(4)\n"
    "v_mfma_f32_32x32x16_bf16 v[144:159], a[224:227], v[96:99], v[144:159]\n"
    "ds_read_b64_tr_b16 a[248:249], v9 offset:40960\n"
    "ds_read_b64_tr_b16 a[250:251], v9 offset:41984\n"
    "v_lshl_add_u32 v243, s44, 4, v136\n"
    "v_mul_u32_u24 v243, 72, v243\n"
    "v_lshl_add_u32 v243, v137, 3, v243\n"
    "v_mfma_f32_32x32x16_bf16 v[144:159], a[228:231], v[100:103], v[144:159]\n"
    "ds_read_b64_tr_b16 a[252:253], v9 offset:43008\n"
    "ds_read_b64_tr_b16 a[254:255], v9 offset:44032\n"
    "s_add_u32 m0, s71, 117760\n"
    "s_add_u32 s51, s71, 605184\n"
    "buffer_load_dwordx4 v1, s[36:39], s51 offen lds\n"
    "v_add_u32 v243, 147456, v243\n"
    "v_lshrrev_b32 v247, 3, v0\n"
    "v_mul_u32_u24 v247, 57344, v247\n"
    "v_mfma_f32_32x32x16_bf16 v[144:159], a[232:235], v[104:107], v[144:159]\n"
    "ds_read_b64_tr_b16 a[224:225], v6 offset:32768\n"
    "ds_read_b64_tr_b16 a[226:227], v6 offset:33792\n"
    "v_and_b32 v246, 7, v0\n"
    "v_lshl_add_u32 v247, v246, 4, v247\n"
    "v_mov_b32 v246, 0x80008000\n"
    "v_mfma_f32_32x32x16_bf16 v[144:159], a[236:239], v[108:111], v[144:159]\n"
    "ds_read_b64_tr_b16 a[228:229], v6 offset:34816\n"
    "ds_read_b64_tr_b16 a[230:231], v6 offset:35840\n"
    "s_add_u32 m0, s71, 118784\n"
    "s_add_u32 s51, s71, 606208\n"
    "buffer_load_dwordx4 v1, s[36:39], s51 offen lds\n"
    "v_cmp_gt_u32 vcc, 8, v136\n"
    "s_nop 1\n"
    "v_cndmask_b32 v246, 0, v246, vcc\n"
    "v_and_b32 v244, 1, v137\n"
    "s_waitcnt lgkmcnt(4)\n"
    "v_mfma_f32_32x32x16_bf16 v[144:159], a[240:243], v[112:115], v[144:159]\n"
    "ds_read_b64_tr_b16 a[232:233], v6 offset:36864\n"
    "ds_read_b64_tr_b16 a[234:235], v6 offset:37888\n"
    "v_lshlrev_b32 v244, 4, v244\n"
    "v_lshrrev_b32 v137, 1, v137\n"
    "v_lshl_add_u32 v244, v137, 2, v244\n"
    "v_mfma_f32_32x32x16_bf16 v[144:159], a[244:247], v[116:119], v[144:159]\n"
    "ds_read_b64_tr_b16 a[236:237], v6 offset:38912\n"
    "ds_read_b64_tr_b16 a[238:239], v6 offset:39936\n"
    "buffer_load_dwordx4 a[84:87], v3, s[32:35], s67 offen sc1\n"
    "v_lshrrev_b32 v137, 2, v136\n"
    "v_add_u32 v244, v244, v137\n"
    "v_mfma_f32_32x32x16_bf16 v[144:159], a[248:251], v[120:123], v[144:159]\n"
    "ds_read_b64_tr_b16 a[240:241], v10 offset:0\n"
    "ds_read_b64_tr_b16 a[242:243], v10 offset:1024\n"
    "v_mul_u32_u24 v244, 72, v244\n"
    "v_and_b32 v137, 1, v0\n"
    "v_lshl_add_u32 v244, v137, 3, v244\n"
    "v_mfma_f32_32x32x16_bf16 v[144:159], a[252:255], v[124:127], v[144:159]\n"
    "ds_read_b64_tr_b16 a[244:245], v10 offset:2048\n"
    "ds_read_b64_tr_b16 a[246:247], v10 offset:3072\n"
    "buffer_load_dwordx4 a[88:91], v2, s[32:35], s67 offen offset:2048 sc1\n"
    "v_lshl_add_u32 v244, s44, 4, v244\n"
    "v_bfe_u32 v137, v0, 1, 1\n"
    "s_waitcnt lgkmcnt(4)\n"
    "v_mfma_f32_32x32x16_bf16 v[160:175], a[224:227], v[48:51], v[160:175]\n"
    "s_cmp_lg_u32 s44, 0\n"
    "s_cbranch_scc1 BACK10_%=\n"
    "s_waitcnt vmcnt(13)\n"
    "v_cmp_ne_u64 vcc, s[60:61], v[240:241]\n"
    "s_nop 1\n"
    "s_cmp_lg_u64 vcc, 0\n"
    "s_cbranch_scc1 SPIN8_%=\n"
    "BACK10_%=:\n"
    "s_waitcnt vmcnt(13)\n"
    "s_barrier\n"
    "s_cmp_lg_u32 s44, 0\n"
    "s_cbranch_scc1 NP11_%=\n"
    "s_mov_b64 exec, 1\n"
    "buffer_store_dwordx2 v[222:223], off, s[32:35], s3 sc1\n"
    "s_mov_b64 exec, -1\n"
    "NP11_%=:\n"
    "ds_read_b64_tr_b16 a[248:249], v10 offset:28672\n"
    "ds_read_b64_tr_b16 a[250:251], v10 offset:29696\n"
    "v_mul_u32_u24 v137, 4608, v137\n"
    "v_add_u32 v244, 147456, v244\n"
    "v_mfma_f32_32x32x16_bf16 v[160:175], a[228:231], v[52:55], v[160:175]\n"
    "ds_read_b64_tr_b16 a[252:253], v10 offset:30720\n"
    "ds_read_b64_tr_b16 a[254:255], v10 offset:31744\n"
    "s_lshl_b32 s59, s44, 11\n"
    "s_add_u32 m0, s59, 135168\n"
    "s_nop 0\n"
    "buffer_load_dwordx4 v250, s[32:35], s98 offen sc1 lds\n"
    "v_sub_u32 v245, v244, v137\n"
    "v_add_u32 v245, 4608, v245\n"
    "v_add_u32 v244, v244, v137\n"
    "v_mfma_f32_32x32x16_bf16 v[160:175], a[232:235], v[56:59], v[160:175]\n"
    "ds_read_b64_tr_b16 a[224:225], v10 offset:4096\n"
    "ds_read_b64_tr_b16 a[226:227], v10 offset:5120\n"
    "v_mul_f32 v212, v14, v144\n"
    "v_exp_f32 v212, v212\n"
    "v_mul_f32 v213, v14, v145\n"
    "v_mfma_f32_32x32x16_bf16 v[160:175], a[236:239], v[60:63], v[160:175]\n"
    "ds_read_b64_tr_b16 a[228:229], v10 offset:6144\n"
    "ds_read_b64_tr_b16 a[230:231], v10 offset:7168\n"
    "s_add_u32 m0, s59, 135168\n"
    "s_nop 0\n"
    "buffer_load_dwordx4 v251, s[32:35], s98 offen offset:1024 sc1 lds\n"
    "v_exp_f32 v213, v213\n"
    "v_mul_f32 v152, v144, v152\n"
    "v_fma_f32 v212, v212, v15, v15\n"
    "s_waitcnt lgkmcnt(4)\n"
    "v_mfma_f32_32x32x16_bf16 v[160:175], a[240:243], v[64:67], v[160:175]\n"
    "ds_read_b64_tr_b16 a[232:233], v10 offset:32768\n"
    "ds_read_b64_tr_b16 a[234:235], v10 offset:33792\n"
    "v_rcp_f32 v212, v212\n"
    "v_fma_f32 v213, v213, v15, v15\n"
    "v_mfma_f32_32x32x16_bf16 v[160:175], a[244:247], v[68:71], v[160:175]\n"
    "ds_read_b64_tr_b16 a[236:237], v10 offset:34816\n"
    "ds_read_b64_tr_b16 a[238:239], v10 offset:35840\n"
    "buffer_load_dwordx4 v[248:251], v1, s[32:35], s99 offen offset:0 sc1\n"
    "v_rcp_f32 v213, v213\n"
    "v_mul_f32 v153, v145, v153\n"
    "v_mul_f32 v152, v152, v212\n"
    "v_mfma_f32_32x32x16_bf16 v[160:175], a[248:251], v[72:75], v[160:175]\n"
    "ds_read_b64_tr_b16 a[240:241], v10 offset:8192\n"
    "ds_read_b64_tr_b16 a[242:243], v10 offset:9216\n"
    "v_mul_f32 v153, v153, v213\n"
    "v_cvt_pk_bf16_f32 v144, v152, v153\n"
    "v_mul_f32 v214, v14, v146\n"
    "v_mfma_f32_32x32x16_bf16 v[160:175], a[252:255], v[76:79], v[160:175]\n"
    "ds_read_b64_tr_b16 a[244:245], v10 offset:10240\n"
    "ds_read_b64_tr_b16 a[246:247], v10 offset:11264\n"
    "buffer_load_dwordx4 v[252:255], v1, s[32:35], s99 offen offset:1024 sc1\n"
    "v_exp_f32 v214, v214\n"
    "v_mul_f32 v215, v14, v147\n"
    "v_exp_f32 v215, v215\n"
    "s_waitcnt lgkmcnt(4)\n"
    "v_mfma_f32_32x32x16_bf16 v[160:175], a[224:227], v[80:83], v[160:175]\n"
    "ds_read_b64_tr_b16 a[248:249], v10 offset:36864\n"
    "ds_read_b64_tr_b16 a[250:251], v10 offset:37888\n"
    "v_mul_f32 v154, v146, v154\n"
    "v_fma_f32 v214, v214, v15, v15\n"
    "v_rcp_f32 v214, v214\n"
    "v_mfma_f32_32x32x16_bf16 v[160:175], a[228:231], v[84:87], v[160:175]\n"
    "ds_read_b64_tr_b16 a[252:253], v10 offset:38912\n"
    "ds_read_b64_tr_b16 a[254:255], v10 offset:39936\n"
    "buffer_load_dwordx4 a[92:95], v3, s[32:35], s67 offen offset:2048 sc1\n"
    "v_fma_f32 v215, v215, v15, v15\n"
    "v_rcp_f32 v215, v215\n"
    "v_mul_f32 v155, v147, v155\n"
    "v_mfma_f32_32x32x16_bf16 v[160:175], a[232:235], v[88:91], v[160:175]\n"
    "ds_read_b64_tr_b16 a[224:225], v10 offset:12288\n"
    "ds_read_b64_tr_b16 a[226:227], v10 offset:13312\n"
    "v_mul_f32 v154, v154, v214\n"
    "v_mul_f32 v155, v155, v215\n"
    "v_cvt_pk_bf16_f32 v145, v154, v155\n"
    "v_mul_f32 v212, v14, v148\n"
    "v_mfma_f32_32x32x16_bf16 v[160:175], a[236:239], v[92:95], v[160:175]\n"
    "ds_read_b64_tr_b16 a[228:229], v10 offset:14336\n"
    "ds_read_b64_tr_b16 a[230:231], v10 offset:15360\n"
    "s_add_u32 s67, s67, 4096\n"
    "buffer_load_dwordx4 a[96:99], v2, s[32:35], s67 offen sc1\n"
    "v_exp_f32 v212, v212\n"
    "v_mul_f32 v213, v14, v149\n"
    "s_waitcnt lgkmcnt(4)\n"
    "v_mfma_f32_32x32x16_bf16 v[160:175], a[240:243], v[96:99], v[160:175]\n"
    "ds_read_b64_tr_b16 a[232:233], v10 offset:40960\n"
    "ds_read_b64_tr_b16 a[234:235], v10 offset:41984\n"
    "v_exp_f32 v213, v213\n"
    "v_mul_f32 v156, v148, v156\n"
    "v_fma_f32 v212, v212, v15, v15\n"
    "v_mfma_f32_32x32x16_bf16 v[160:175], a[244:247], v[100:103], v[160:175]\n"
    "ds_read_b64_tr_b16 a[236:237], v10 offset:43008\n"
    "ds_read_b64_tr_b16 a[238:239], v10 offset:44032\n"
    "buffer_load_dwordx4 a[100:103], v3, s[32:35], s67 offen sc1\n"
    "v_rcp_f32 v212, v212\n"
    "v_fma_f32 v213, v213, v15, v15\n"
    "v_rcp_f32 v213, v213\n"
    "v_mfma_f32_32x32x16_bf16 v[160:175], a[248:251], v[104:107], v[160:175]\n"
    "ds_read_b64_tr_b16 a[240:241], v7 offset:32768\n"
    "ds_read_b64_tr_b16 a[242:243], v7 offset:33792\n"
    "v_mul_f32 v157, v149, v157\n"
    "v_mul_f32 v156, v156, v212\n"
    "v_mul_f32 v157, v157, v213\n"
    "v_cvt_pk_bf16_f32 v146, v156, v157\n"
    "v_mfma_f32_32x32x16_bf16 v[160:175], a[252:255], v[108:111], v[160:175]\n"
    "ds_read_b64_tr_b16 a[244:245], v7 offset:34816\n"
    "ds_read_b64_tr_b16 a[246:247], v7 offset:35840\n"
    "buffer_load_dwordx4 a[104:107], v2, s[32:35], s67 offen offset:2048 sc1\n"
    "v_mul_f32 v214, v14, v150\n"
    "v_exp_f32 v214, v214\n"
    "v_mul_f32 v215, v14, v151\n"
    "s_waitcnt lgkmcnt(4)\n"
    "v_mfma_f32_32x32x16_bf16 v[160:175], a[224:227], v[112:115], v[160:175]\n"
    "ds_read_b64_tr_b16 a[248:249], v7 offset:36864\n"
    "ds_read_b64_tr_b16 a[250:251], v7 offset:37888\n"
    "v_exp_f32 v215, v215\n"
    "v_mul_f32 v158, v150, v158\n"
    "v_fma_f32 v214, v214, v15, v15\n"
    "v_mfma_f32_32x32x16_bf16 v[160:175], a[228:231], v[116:119], v[160:175]\n"
    "ds_read_b64_tr_b16 a[252:253], v7 offset:38912\n"
    "ds_read_b64_tr_b16 a[254:255], v7 offset:39936\n"
    "buffer_load_dwordx4 a[108:111], v3, s[32:35], s67 offen offset:2048 sc1\n"
    "v_rcp_f32 v214, v214\n"
    "v_fma_f32 v215, v215, v15, v15\n"
    "v_mfma_f32_32x32x16_bf16 v[160:175], a[232:235], v[120:123], v[160:175]\n"
    "ds_read_b64_tr_b16 a[224:225], v11 offset:0\n"
    "ds_read_b64_tr_b16 a[226:227], v11 offset:1024\n"
    "v_rcp_f32 v215, v215\n"
    "v_mul_f32 v159, v151, v159\n"
    "v_mul_f32 v158, v158, v214\n"
    "v_mfma_f32_32x32x16_bf16 v[160:175], a[236:239], v[124:127], v[160:175]\n"
    "ds_read_b64_tr_b16 a[228:229], v11 offset:2048\n"
    "ds_read_b64_tr_b16 a[230:231], v11 offset:3072\n"
    "s_add_u32 s67, s67, 4096\n"
    "buffer_load_dwordx4 a[112:115], v2, s[32:35], s67 offen sc1\n"
    "v_mul_f32 v159, v159, v215\n"
    "v_cvt_pk_bf16_f32 v147, v158, v159\n"
    "s_add_u32 s51, s66, 28672\n"
    "buffer_store_dwordx4 v[144:147], v1, s[32:35], s51 offen sc1\n"
    "s_waitcnt lgkmcnt(4)\n"
    "v_mfma_f32_32x32x16_bf16 v[176:191], a[240:243], v[48:51], v[176:191]\n"
    "ds_read_b64_tr_b16 a[232:233], v11 offset:28672\n"
    "ds_read_b64_tr_b16 a[234:235], v11 offset:29696\n"
    "v_mfma_f32_32x32x16_bf16 v[176:191], a[244:247], v[52:55], v[176:191]\n"
    "ds_read_b64_tr_b16 a[236:237], v11 offset:30720\n"
    "ds_read_b64_tr_b16 a[238:239], v11 offset:31744\n"
    "buffer_load_dwordx4 a[116:119], v3, s[32:35], s67 offen sc1\n"
    "v_mfma_f32_32x32x16_bf16 v[176:191], a[248:251], v[56:59], v[176:191]\n"
    "ds_read_b64_tr_b16 a[240:241], v11 offset:4096\n"
    "ds_read_b64_tr_b16 a[242:243], v11 offset:5120\n"
    "v_mul_f32 v212, v14, v160\n"
    "v_exp_f32 v212, v212\n"
    "v_mul_f32 v213, v14, v161\n"
    "v_mfma_f32_32x32x16_bf16 v[176:191], a[252:255], v[60:63], v[176:191]\n"
    "ds_read_b64_tr_b16 a[244:245], v11 offset:6144\n"
    "ds_read_b64_tr_b16 a[246:247], v11 offset:7168\n"
    "buffer_load_dwordx4 a[120:123], v2, s[32:35], s67 offen offset:2048 sc1\n"
    "v_exp_f32 v213, v213\n"
    "v_mul_f32 v168, v160, v168\n"
    "v_fma_f32 v212, v212, v15, v15\n"
    "s_waitcnt lgkmcnt(4)\n"
    "v_mfma_f32_32x32x16_bf16 v[176:191], a[224:227], v[64:67], v[176:191]\n"
    "ds_read_b64_tr_b16 a[248:249], v11 offset:32768\n"
    "ds_read_b64_tr_b16 a[250:251], v11 offset:33792\n"
    "v_rcp_f32 v212, v212\n"
    "v_fma_f32 v213, v213, v15, v15\n"
    "v_mfma_f32_32x32x16_bf16 v[176:191], a[228:231], v[68:71], v[176:191]\n"
    "ds_read_b64_tr_b16 a[252:253], v11 offset:34816\n"
    "ds_read_b64_tr_b16 a[254:255], v11 offset:35840\n"
    "buffer_load_dwordx4 a[124:127], v3, s[32:35], s67 offen offset:2048 sc1\n"
    "v_rcp_f32 v213, v213\n"
    "v_mul_f32 v169, v161, v169\n"
    "v_mul_f32 v168, v168, v212\n"
    "v_mfma_f32_32x32x16_bf16 v[176:191], a[232:235], v[72:75], v[176:191]\n"
    "ds_read_b64_tr_b16 a[224:225], v11 offset:8192\n"
    "ds_read_b64_tr_b16 a[226:227], v11 offset:9216\n"
    "v_mul_f32 v169, v169, v213\n"
    "v_cvt_pk_bf16_f32 v160, v168, v169\n"
    "v_mul_f32 v214, v14, v162\n"
    "v_mfma_f32_32x32x16_bf16 v[176:191], a[236:239], v[76:79], v[176:191]\n"
    "ds_read_b64_tr_b16 a[228:229], v11 offset:10240\n"
    "ds_read_b64_tr_b16 a[230:231], v11 offset:11264\n"
    "s_add_u32 s67, s67, 4096\n"
    "buffer_load_dwordx4 a[128:131], v2, s[32:35], s67 offen sc1\n"
    "v_exp_f32 v214, v214\n"
    "v_mul_f32 v215, v14, v163\n"
    "v_exp_f32 v215, v215\n"
    "s_waitcnt lgkmcnt(4)\n"
    "v_mfma_f32_32x32x16_bf16 v[176:191], a[240:243], v[80:83], v[176:191]\n"
    "ds_read_b64_tr_b16 a[232:233], v11 offset:36864\n"
    "ds_read_b64_tr_b16 a[234:235], v11 offset:37888\n"
    "v_mul_f32 v170, v162, v170\n"
    "v_fma_f32 v214, v214, v15, v15\n"
    "v_rcp_f32 v214, v214\n"
    "v_mfma_f32_32x32x16_bf16 v[176:191], a[244:247], v[84:87], v[176:191]\n"
    "ds_read_b64_tr_b16 a[236:237], v11 offset:38912\n"
    "ds_read_b64_tr_b16 a[238:239], v11 offset:39936\n"
    "buffer_load_dwordx4 a[132:135], v3, s[32:35], s67 offen sc1\n"
    "v_fma_f32 v215, v215, v15, v15\n"
    "v_rcp_f32 v215, v215\n"
    "v_mul_f32 v171, v163, v171\n"
    "v_mfma_f32_32x32x16_bf16 v[176:191], a[248:251], v[88:91], v[176:191]\n"
    "ds_read_b64_tr_b16 a[240:241], v11 offset:12288\n"
    "ds_read_b64_tr_b16 a[242:243], v11 offset:13312\n"
    "v_mul_f32 v170, v170, v214\n"
    "v_mul_f32 v171, v171, v215\n"
    "v_cvt_pk_bf16_f32 v161, v170, v171\n"
    "v_mul_f32 v212, v14, v164\n"
    "v_mfma_f32_32x32x16_bf16 v[176:191], a[252:255], v[92:95], v[176:191]\n"
    "ds_read_b64_tr_b16 a[244:245], v11 offset:14336\n"
    "ds_read_b64_tr_b16 a[246:247], v11 offset:15360\n"
    "buffer_load_dwordx4 a[136:139], v2, s[32:35], s67 offen offset:2048 sc1\n"
    "v_exp_f32 v212, v212\n"
    "v_mul_f32 v213, v14, v165\n"
    "s_waitcnt lgkmcnt(4)\n"
    "v_mfma_f32_32x32x16_bf16 v[176:191], a[224:227], v[96:99], v[176:191]\n"
    "ds_read_b64_tr_b16 a[248:249], v11 offset:40960\n"
    "ds_read_b64_tr_b16 a[250:251], v11 offset:41984\n"
    "v_exp_f32 v213, v213\n"
    "v_mul_f32 v172, v164, v172\n"
    "v_fma_f32 v212, v212, v15, v15\n"
    "v_mfma_f32_32x32x16_bf16 v[176:191], a[228:231], v[100:103], v[176:191]\n"
    "ds_read_b64_tr_b16 a[252:253], v11 offset:43008\n"
    "ds_read_b64_tr_b16 a[254:255], v11 offset:44032\n"
    "buffer_load_dwordx4 a[140:143], v3, s[32:35], s67 offen offset:2048 sc1\n"
    "v_rcp_f32 v212, v212\n"
    "v_fma_f32 v213, v213, v15, v15\n"
    "v_rcp_f32 v213, v213\n"
    "v_mfma_f32_32x32x16_bf16 v[176:191], a[232:235], v[104:107], v[176:191]\n"
    "ds_read_b64_tr_b16 a[224:225], v8 offset:32768\n"
    "ds_read_b64_tr_b16 a[226:227], v8 offset:33792\n"
    "v_mul_f32 v173, v165, v173\n"
    "v_mul_f32 v172, v172, v212\n"
    "v_mul_f32 v173, v173, v213\n"
    "v_cvt_pk_bf16_f32 v162, v172, v173\n"
    "v_mfma_f32_32x32x16_bf16 v[176:191], a[236:239], v[108:111], v[176:191]\n"
    "ds_read_b64_tr_b16 a[228:229], v8 offset:34816\n"
    "ds_read_b64_tr_b16 a[230:231], v8 offset:35840\n"
    "s_add_u32 s67, s67, 4096\n"
    "buffer_load_dwordx4 a[144:147], v2, s[32:35], s67 offen sc1\n"
    "v_mul_f32 v214, v14, v166\n"
    "v_exp_f32 v214, v214\n"
    "v_mul_f32 v215, v14, v167\n"
    "s_waitcnt lgkmcnt(4)\n"
    "v_mfma_f32_32x32x16_bf16 v[176:191], a[240:243], v[112:115], v[176:191]\n"
    "ds_read_b64_tr_b16 a[232:233], v8 offset:36864\n"
    "ds_read_b64_tr_b16 a[234:235], v8 offset:37888\n"
    "v_exp_f32 v215, v215\n"
    "v_mul_f32 v174, v166, v174\n"
    "v_fma_f32 v214, v214, v15, v15\n"
    "v_mfma_f32_32x32x16_bf16 v[176:191], a[244:247], v[116:119], v[176:191]\n"
    "ds_read_b64_tr_b16 a[236:237], v8 offset:38912\n"
    "ds_read_b64_tr_b16 a[238:239], v8 offset:39936\n"
    "buffer_load_dwordx4 a[148:151], v3, s[32:35], s67 offen sc1\n"
    "v_rcp_f32 v214, v214\n"
    "v_fma_f32 v215, v215, v15, v15\n"
    "v_mfma_f32_32x32x16_bf16 v[176:191], a[248:251], v[120:123], v[176:191]\n"
    "ds_read_b64_tr_b16 a[240:241], v12 offset:0\n"
    "ds_read_b64_tr_b16 a[242:243], v12 offset:1024\n"
    "v_rcp_f32 v215, v215\n"
    "v_mul_f32 v175, v167, v175\n"
    "v_mul_f32 v174, v174, v214\n"
    "v_mfma_f32_32x32x16_bf16 v[176:191], a[252:255], v[124:127], v[176:191]\n"
    "ds_read_b64_tr_b16 a[244:245], v12 offset:2048\n"
    "ds_read_b64_tr_b16 a[246:247], v12 offset:3072\n"
    "buffer_load_dwordx4 a[152:155], v2, s[32:35], s67 offen offset:2048 sc1\n"
    "v_mul_f32 v175, v175, v215\n"
    "v_cvt_pk_bf16_f32 v163, v174, v175\n"
    "s_add_u32 s51, s66, 86016\n"
    "buffer_store_dwordx4 v[160:163], v1, s[32:35], s51 offen sc1\n"
    "s_waitcnt lgkmcnt(4)\n"
    "v_mfma_f32_32x32x16_bf16 v[192:207], a[224:227], v[48:51], v[192:207]\n"
    "s_cmp_lg_u32 s44, 0\n"
    "s_cbranch_scc1 NQ12_%=\n"
    "buffer_load_dwordx2 v[240:241], v221, s[32:35], 0 offen sc1\n"
    "NQ12_%=:\n"
    "ds_read_b64_tr_b16 a[248:249], v12 offset:28672\n"
    "ds_read_b64_tr_b16 a[250:251], v12 offset:29696\n"
    "v_mfma_f32_32x32x16_bf16 v[192:207], a[228:231], v[52:55], v[192:207]\n"
    "ds_read_b64_tr_b16 a[252:253], v12 offset:30720\n"
    "ds_read_b64_tr_b16 a[254:255], v12 offset:31744\n"
    "buffer_load_dwordx4 a[156:159], v3, s[32:35], s67 offen offset:2048 sc1\n"
    "v_mfma_f32_32x32x16_bf16 v[192:207], a[232:235], v[56:59], v[192:207]\n"
    "ds_read_b64_tr_b16 a[224:225], v12 offset:4096\n"
    "ds_read_b64_tr_b16 a[226:227], v12 offset:5120\n"
    "v_mul_f32 v212, v14, v176\n"
    "v_exp_f32 v212, v212\n"
    "v_mul_f32 v213, v14, v177\n"
    "v_mfma_f32_32x32x16_bf16 v[192:207], a[236:239], v[60:63], v[192:207]\n"
    "ds_read_b64_tr_b16 a[228:229], v12 offset:6144\n"
    "ds_read_b64_tr_b16 a[230:231], v12 offset:7168\n"
    "s_add_u32 s67, s67, 4096\n"
    "buffer_load_dwordx4 a[160:163], v2, s[32:35], s67 offen sc1\n"
    "v_exp_f32 v213, v213\n"
    "v_mul_f32 v184, v176, v184\n"
    "v_fma_f32 v212, v212, v15, v15\n"
    "s_waitcnt lgkmcnt(4)\n"
    "v_mfma_f32_32x32x16_bf16 v[192:207], a[240:243], v[64:67], v[192:207]\n"
    "ds_read_b64_tr_b16 a[232:233], v12 offset:32768\n"
    "ds_read_b64_tr_b16 a[234:235], v12 offset:33792\n"
    "v_rcp_f32 v212, v212\n"
    "v_fma_f32 v213, v213, v15, v15\n"
    "v_mfma_f32_32x32x16_bf16 v[192:207], a[244:247], v[68:71], v[192:207]\n"
    "ds_read_b64_tr_b16 a[236:237], v12 offset:34816\n"
    "ds_read_b64_tr_b16 a[238:239], v12 offset:35840\n"
    "buffer_load_dwordx4 a[164:167], v3, s[32:35], s67 offen sc1\n"
    "v_rcp_f32 v213, v213\n"
    "v_mul_f32 v185, v177, v185\n"
    "v_mul_f32 v184, v184, v212\n"
    "v_mfma_f32_32x32x16_bf16 v[192:207], a[248:251], v[72:75], v[192:207]\n"
    "ds_read_b64_tr_b16 a[240:241], v12 offset:8192\n"
    "ds_read_b64_tr_b16 a[242:243], v12 offset:9216\n"
    "v_mul_f32 v185, v185, v213\n"
    "v_cvt_pk_bf16_f32 v176, v184, v185\n"
    "v_mul_f32 v214, v14, v178\n"
    "v_mfma_f32_32x32x16_bf16 v[192:207], a[252:255], v[76:79], v[192:207]\n"
    "ds_read_b64_tr_b16 a[244:245], v12 offset:10240\n"
    "ds_read_b64_tr_b16 a[246:247], v12 offset:11264\n"
    "buffer_load_dwordx4 a[168:171], v2, s[32:35], s67 offen offset:2048 sc1\n"
    "v_exp_f32 v214, v214\n"
    "v_mul_f32 v215, v14, v179\n"
    "v_exp_f32 v215, v215\n"
    "s_waitcnt lgkmcnt(4)\n"
    "v_mfma_f32_32x32x16_bf16 v[192:207], a[224:227], v[80:83], v[192:207]\n"
    "ds_read_b64_tr_b16 a[248:249], v12 offset:36864\n"
    "ds_read_b64_tr_b16 a[250:251], v12 offset:37888\n"
    "v_mul_f32 v186, v178, v186\n"
    "v_fma_f32 v214, v214, v15, v15\n"
    "v_rcp_f32 v214, v214\n"
    "v_mfma_f32_32x32x16_bf16 v[192:207], a[228:231], v[84:87], v[192:207]\n"
    "ds_read_b64_tr_b16 a[252:253], v12 offset:38912\n"
    "ds_read_b64_tr_b16 a[254:255], v12 offset:39936\n"
    "buffer_load_dwordx4 a[172:175], v3, s[32:35], s67 offen offset:2048 sc1\n"
    "v_fma_f32 v215, v215, v15, v15\n"
    "v_rcp_f32 v215, v215\n"
    "v_mul_f32 v187, v179, v187\n"
    "v_mfma_f32_32x32x16_bf16 v[192:207], a[232:235], v[88:91], v[192:207]\n"
    "ds_read_b64_tr_b16 a[224:225], v12 offset:12288\n"
    "ds_read_b64_tr_b16 a[226:227], v12 offset:13312\n"
    "v_mul_f32 v186, v186, v214\n"
    "v_mul_f32 v187, v187, v215\n"
    "v_cvt_pk_bf16_f32 v177, v186, v187\n"
    "v_mul_f32 v212, v14, v180\n"
    "v_mfma_f32_32x32x16_bf16 v[192:207], a[236:239], v[92:95], v[192:207]\n"
    "ds_read_b64_tr_b16 a[228:229], v12 offset:14336\n"
    "ds_read_b64_tr_b16 a[230:231], v12 offset:15360\n"
    "s_add_u32 s67, s67, 4096\n"
    "buffer_load_dwordx4 a[176:179], v2, s[32:35], s67 offen sc1\n"
    "v_exp_f32 v212, v212\n"
    "v_mul_f32 v213, v14, v181\n"
    "s_waitcnt lgkmcnt(4)\n"
    "v_mfma_f32_32x32x16_bf16 v[192:207], a[240:243], v[96:99], v[192:207]\n"
    "ds_read_b64_tr_b16 a[232:233], v12 offset:40960\n"
    "ds_read_b64_tr_b16 a[234:235], v12 offset:41984\n"
    "v_exp_f32 v213, v213\n"
    "v_mul_f32 v188, v180, v188\n"
    "v_fma_f32 v212, v212, v15, v15\n"
    "v_mfma_f32_32x32x16_bf16 v[192:207], a[244:247], v[100:103], v[192:207]\n"
    "ds_read_b64_tr_b16 a[236:237], v12 offset:43008\n"
    "ds_read_b64_tr_b16 a[238:239], v12 offset:44032\n"
    "buffer_load_dwordx4 a[180:183], v3, s[32:35], s67 offen sc1\n"
    "v_rcp_f32 v212, v212\n"
    "v_fma_f32 v213, v213, v15, v15\n"
    "v_rcp_f32 v213, v213\n"
    "v_mfma_f32_32x32x16_bf16 v[192:207], a[248:251], v[104:107], v[192:207]\n"
    "v_mul_f32 v189, v181, v189\n"
    "v_mul_f32 v188, v188, v212\n"
    "v_mul_f32 v189, v189, v213\n"
    "v_cvt_pk_bf16_f32 v178, v188, v189\n"
    "v_mul_f32 v214, v14, v182\n"
    "v_mfma_f32_32x32x16_bf16 v[192:207], a[252:255], v[108:111], v[192:207]\n"
    "buffer_load_dwordx4 a[184:187], v2, s[32:35], s67 offen offset:2048 sc1\n"
    "v_exp_f32 v214, v214\n"
    "v_mul_f32 v215, v14, v183\n"
    "s_waitcnt lgkmcnt(0)\n"
    "v_mfma_f32_32x32x16_bf16 v[192:207], a[224:227], v[112:115], v[192:207]\n"
    "v_exp_f32 v215, v215\n"
    "v_mul_f32 v190, v182, v190\n"
    "v_fma_f32 v214, v214, v15, v15\n"
    "v_mfma_f32_32x32x16_bf16 v[192:207], a[228:231], v[116:119], v[192:207]\n"
    "buffer_load_dwordx4 a[188:191], v3, s[32:35], s67 offen offset:2048 sc1\n"
    "v_rcp_f32 v214, v214\n"
    "v_fma_f32 v215, v215, v15, v15\n"
    "v_mfma_f32_32x32x16_bf16 v[192:207], a[232:235], v[120:123], v[192:207]\n"
    "v_rcp_f32 v215, v215\n"
    "v_mul_f32 v191, v183, v191\n"
    "v_mul_f32 v190, v190, v214\n"
    "v_mul_f32 v191, v191, v215\n"
    "v_mfma_f32_32x32x16_bf16 v[192:207], a[236:239], v[124:127], v[192:207]\n"
    "s_add_u32 s67, s67, 4096\n"
    "buffer_load_dwordx4 a[192:195], v2, s[32:35], s67 offen sc1\n"
    "v_cvt_pk_bf16_f32 v179, v190, v191\n"
    "s_add_u32 s51, s66, 143360\n"
    "buffer_store_dwordx4 v[176:179], v1, s[32:35], s51 offen sc1\n"
    "s_cmp_lg_u32 s44, 0\n"
    "s_cbranch_scc1 BACK15_%=\n"
    "s_waitcnt vmcnt(11)\n"
    "v_cmp_ne_u64 vcc, s[60:61], v[240:241]\n"
    "s_nop 1\n"
    "s_cmp_lg_u64 vcc, 0\n"
    "s_cbranch_scc1 SPIN13_%=\n"
    "BACK15_%=:\n"
    "s_lshl_b32 s51, s58, 10\n"
    "v_add_u32 v11, s51, v1\n"
    "v_xor_b32 v12, 0x400, v11\n"
    "; ---- FFT ----\n"
    "v_mov_b32 v208, v242\n"
    "v_xor_b32 v209, 64, v242\n"
    "v_lshl_add_u32 v210, s44, 12, v13\n"
    "s_waitcnt vmcnt(31)\n"
    "s_barrier\n"
    "ds_read_b128 v[128:131], v210 offset:0\n"
    "ds_read_b128 v[132:135], v210 offset:1024\n"
    "ds_read_b128 a[224:227], v208 offset:0\n"
    "ds_read_b128 a[232:235], v208 offset:4096\n"
    "ds_read_b128 a[240:243], v208 offset:2048\n"
    "ds_read_b128 a[248:251], v208 offset:6144\n"
    "ds_read_b128 v[136:139], v210 offset:2048\n"
    "ds_read_b128 v[140:143], v210 offset:3072\n"
    "ds_read_b128 a[228:231], v209 offset:0\n"
    "ds_read_b128 a[236:239], v209 offset:4096\n"
    "ds_read_b128 a[244:247], v209 offset:2048\n"
    "ds_read_b128 a[252:255], v209 offset:6144\n"
    "s_mul_i32 s47, s56, 28672\n"
    "s_lshl_b32 s71, s44, 10\n"
    "s_add_u32 s47, s47, s71\n"
    "buffer_load_dwordx4 v[16:19], v11, s[32:35], s68 offen sc1\n"
    "buffer_load_dwordx4 v[20:23], v12, s[32:35], s68 offen sc1\n"
    "v_mul_f32 v212, v14, v192\n"
    "v_exp_f32 v212, v212\n"
    "v_mul_f32 v213, v14, v193\n"
    "v_exp_f32 v213, v213\n"
    "v_mul_f32 v200, v192, v200\n"
    "v_fma_f32 v212, v212, v15, v15\n"
    "v_rcp_f32 v212, v212\n"
    "v_fma_f32 v213, v213, v15, v15\n"
    "v_rcp_f32 v213, v213\n"
    "v_mul_f32 v201, v193, v201\n"
    "v_mul_f32 v200, v200, v212\n"
    "v_mul_f32 v201, v201, v213\n"
    "v_cvt_pk_bf16_f32 v192, v200, v201\n"
    "v_mul_f32 v214, v14, v194\n"
    "v_exp_f32 v214, v214\n"
    "v_mul_f32 v215, v14, v195\n"
    "v_exp_f32 v215, v215\n"
    "v_mul_f32 v202, v194, v202\n"
    "v_fma_f32 v214, v214, v15, v15\n"
    "v_rcp_f32 v214, v214\n"
    "v_fma_f32 v215, v215, v15, v15\n"
    "v_rcp_f32 v215, v215\n"
    "v_mul_f32 v203, v195, v203\n"
    "v_mul_f32 v202, v202, v214\n"
    "s_waitcnt lgkmcnt(9)\n"
    "v_mfma_f32_16x16x32_bf16 v[144:147], a[224:227], v[128:131], 0\n"
    "v_mul_f32 v203, v203, v215\n"
    "v_cvt_pk_bf16_f32 v193, v202, v203\n"
    "s_waitcnt lgkmcnt(8)\n"
    "v_mfma_f32_16x16x32_bf16 v[152:155], a[232:235], v[128:131], 0\n"
    "v_mul_f32 v212, v14, v196\n"
    "v_exp_f32 v212, v212\n"
    "s_waitcnt lgkmcnt(7)\n"
    "v_mfma_f32_16x16x32_bf16 v[148:151], a[240:243], v[128:131], 0\n"
    "buffer_load_dwordx4 v[24:27], v11, s[32:35], s68 offen offset:2048 sc1\n"
    "v_mul_f32 v213, v14, v197\n"
    "v_exp_f32 v213, v213\n"
    "s_waitcnt lgkmcnt(6)\n"
    "v_mfma_f32_16x16x32_bf16 v[156:159], a[248:251], v[128:131], 0\n"
    "v_mul_f32 v204, v196, v204\n"
    "v_fma_f32 v212, v212, v15, v15\n"
    "v_mfma_f32_16x16x32_bf16 v[144:147], a[232:235], v[132:135], v[144:147]\n"
    "buffer_load_dwordx4 v[28:31], v12, s[32:35], s68 offen offset:2048 sc1\n"
    "v_rcp_f32 v212, v212\n"
    "v_fma_f32 v213, v213, v15, v15\n"
    "v_mfma_f32_16x16x32_bf16 v[160:163], a[224:227], v[132:135], 0\n"
    "v_rcp_f32 v213, v213\n"
    "v_mul_f32 v205, v197, v205\n"
    "v_mfma_f32_16x16x32_bf16 v[148:151], a[248:251], v[132:135], v[148:151]\n"
    "s_add_u32 s68, s68, 4096\n"
    "buffer_load_dwordx4 v[32:35], v11, s[32:35], s68 offen sc1\n"
    "v_mul_f32 v204, v204, v212\n"
    "v_mul_f32 v205, v205, v213\n"
    "v_mfma_f32_16x16x32_bf16 v[164:167], a[240:243], v[132:135], 0\n"
    "v_cvt_pk_bf16_f32 v194, v204, v205\n"
    "v_mul_f32 v214, v14, v198\n"
    "s_waitcnt lgkmcnt(3)\n"
    "v_mfma_f32_16x16x32_bf16 v[144:147], a[228:231], v[136:139], v[144:147]\n"
    "buffer_load_dwordx4 v[36:39], v12, s[32:35], s68 offen sc1\n"
    "v_exp_f32 v214, v214\n"
    "v_mul_f32 v215, v14, v199\n"
    "s_waitcnt lgkmcnt(2)\n"
    "v_mfma_f32_16x16x32_bf16 v[152:155], a[236:239], v[136:139], v[152:155]\n"
    "v_exp_f32 v215, v215\n"
    "v_mul_f32 v206, v198, v206\n"
    "s_waitcnt lgkmcnt(1)\n"
    "v_mfma_f32_16x16x32_bf16 v[148:151], a[244:247], v[136:139], v[148:151]\n"
    "buffer_load_dwordx4 v[40:43], v11, s[32:35], s68 offen offset:2048 sc1\n"
    "v_fma_f32 v214, v214, v15, v15\n"
    "v_rcp_f32 v214, v214\n"
    "s_waitcnt lgkmcnt(0)\n"
    "v_mfma_f32_16x16x32_bf16 v[156:159], a[252:255], v[136:139], v[156:159]\n"
    "v_fma_f32 v215, v215, v15, v15\n"
    "v_rcp_f32 v215, v215\n"
    "v_mfma_f32_16x16x32_bf16 v[144:147], a[236:239], v[140:143], v[144:147]\n"
    "buffer_load_dwordx4 v[44:47], v12, s[32:35], s68 offen offset:2048 sc1\n"
    "v_mul_f32 v207, v199, v207\n"
    "v_mul_f32 v206, v206, v214\n"
    "v_mfma_f32_16x16x32_bf16 v[160:163], a[228:231], v[140:143], v[160:163]\n"
    "v_mul_f32 v207, v207, v215\n"
    "v_cvt_pk_bf16_f32 v195, v206, v207\n"
    "v_mfma_f32_16x16x32_bf16 v[148:151], a[252:255], v[140:143], v[148:151]\n"
    "s_add_u32 s51, s66, 200704\n"
    "buffer_store_dwordx4 v[192:195], v1, s[32:35], s51 offen sc1\n"
    "s_add_u32 s68, s68, 4096\n"
    "buffer_load_dwordx4 v[48:51], v11, s[32:35], s68 offen sc1\n"
    "v_mfma_f32_16x16x32_bf16 v[164:167], a[244:247], v[140:143], v[164:167]\n"
    "ds_read_b128 a[224:227], v13 offset:16384\n"
    "ds_read_b128 a[228:231], v13 offset:17408\n"
    "ds_read_b128 a[232:235], v13 offset:18432\n"
    "ds_read_b128 a[236:239], v13 offset:19456\n"
    "v_pk_add_f32 v[152:153], v[152:153], v[160:161] neg_lo:[0,1] neg_hi:[0,1]\n"
    "v_pk_add_f32 v[154:155], v[154:155], v[162:163] neg_lo:[0,1] neg_hi:[0,1]\n"
    "s_nop 1\n"
    "v_pk_add_f32 v[156:157], v[156:157], v[164:165] neg_lo:[0,1] neg_hi:[0,1]\n"
    "v_pk_add_f32 v[158:159], v[158:159], v[166:167] neg_lo:[0,1] neg_hi:[0,1]\n"
    "buffer_load_dwordx4 v[52:55], v12, s[32:35], s68 offen sc1\n"
    "v_pk_mul_f32 v[128:129], v[144:145], v[224:225]\n"
    "v_pk_mul_f32 v[136:137], v[152:153], v[224:225]\n"
    "v_pk_fma_f32 v[128:129], v[152:153], v[232:233], v[128:129]\n"
    "v_pk_fma_f32 v[136:137], v[144:145], v[232:233], v[136:137] neg_lo:[1,0,0] neg_hi:[1,0,0]\n"
    "v_pk_mul_f32 v[130:131], v[146:147], v[226:227]\n"
    "v_pk_mul_f32 v[138:139], v[154:155], v[226:227]\n"
    "v_pk_fma_f32 v[130:131], v[154:155], v[234:235], v[130:131]\n"
    "v_pk_fma_f32 v[138:139], v[146:147], v[234:235], v[138:139] neg_lo:[1,0,0] neg_hi:[1,0,0]\n"
    "v_pk_mul_f32 v[132:133], v[148:149], v[228:229]\n"
    "v_pk_mul_f32 v[140:141], v[156:157], v[228:229]\n"
    "v_pk_fma_f32 v[132:133], v[156:157], v[236:237], v[132:133]\n"
    "v_pk_fma_f32 v[140:141], v[148:149], v[236:237], v[140:141] neg_lo:[1,0,0] neg_hi:[1,0,0]\n"
    "buffer_load_dwordx4 v[56:59], v11, s[32:35], s68 offen offset:2048 sc1\n"
    "v_pk_mul_f32 v[134:135], v[150:151], v[230:231]\n"
    "v_pk_mul_f32 v[142:143], v[158:159], v[230:231]\n"
    "v_pk_fma_f32 v[134:135], v[158:159], v[238:239], v[134:135]\n"
    "v_pk_fma_f32 v[142:143], v[150:151], v[238:239], v[142:143] neg_lo:[1,0,0] neg_hi:[1,0,0]\n"
    "v_cvt_pk_bf16_f32 v168, v128, v129\n"
    "v_cvt_pk_bf16_f32 v169, v130, v131\n"
    "v_cvt_pk_bf16_f32 v170, v132, v133\n"
    "v_cvt_pk_bf16_f32 v171, v134, v135\n"
    "v_cvt_pk_bf16_f32 v172, v136, v137\n"
    "v_cvt_pk_bf16_f32 v173, v138, v139\n"
    "v_cvt_pk_bf16_f32 v174, v140, v141\n"
    "v_cvt_pk_bf16_f32 v175, v142, v143\n"
    "buffer_load_dwordx4 v[60:63], v12, s[32:35], s68 offen offset:2048 sc1\n"
    "v_cvt_pk_bf16_f32 v176, -v128, -v129\n"
    "v_cvt_pk_bf16_f32 v177, -v130, -v131\n"
    "v_cvt_pk_bf16_f32 v178, -v132, -v133\n"
    "v_cvt_pk_bf16_f32 v179, -v134, -v135\n"
    "s_waitcnt lgkmcnt(3)\n"
    "v_mfma_f32_16x16x32_bf16 v[144:147], a[224:227], v[168:171], 0\n"
    "s_waitcnt vmcnt(43)\n"
    "v_lshlrev_b32 v180, 16, v248\n"
    "v_and_b32 v188, s77, v248\n"
    "v_mfma_f32_16x16x32_bf16 v[152:155], a[224:227], v[172:175], 0\n"
    "s_add_u32 s68, s68, 4096\n"
    "buffer_load_dwordx4 v[64:67], v11, s[32:35], s68 offen sc1\n"
    "v_lshlrev_b32 v181, 16, v249\n"
    "v_and_b32 v189, s77, v249\n"
    "s_waitcnt lgkmcnt(1)\n"
    "v_mfma_f32_16x16x32_bf16 v[148:151], a[232:235], v[168:171], 0\n"
    "v_lshlrev_b32 v182, 16, v250\n"
    "v_and_b32 v190, s77, v250\n"
    "v_mfma_f32_16x16x32_bf16 v[156:159], a[232:235], v[172:175], 0\n"
    "buffer_load_dwordx4 v[68:71], v12, s[32:35], s68 offen sc1\n"
    "v_lshlrev_b32 v183, 16, v251\n"
    "v_and_b32 v191, s77, v251\n"
    "v_mfma_f32_16x16x32_bf16 v[144:147], a[228:231], v[172:175], v[144:147]\n"
    "s_waitcnt vmcnt(44)\n"
    "v_lshlrev_b32 v184, 16, v252\n"
    "v_and_b32 v192, s77, v252\n"
    "v_mfma_f32_16x16x32_bf16 v[152:155], a[228:231], v[176:179], v[152:155]\n"
    "v_lshlrev_b32 v185, 16, v253\n"
    "buffer_load_dwordx4 v[72:75], v11, s[32:35], s68 offen offset:2048 sc1\n"
    "v_and_b32 v193, s77, v253\n"
    "s_waitcnt lgkmcnt(0)\n"
    "v_mfma_f32_16x16x32_bf16 v[148:151], a[236:239], v[172:175], v[148:151]\n"
    "v_lshlrev_b32 v186, 16, v254\n"
    "v_and_b32 v194, s77, v254\n"
    "v_mfma_f32_16x16x32_bf16 v[156:159], a[236:239], v[176:179], v[156:159]\n"
    "v_lshlrev_b32 v187, 16, v255\n"
    "v_and_b32 v195, s77, v255\n"
    "buffer_load_dwordx4 v[76:79], v12, s[32:35], s68 offen offset:2048 sc1\n"
    "v_pk_mul_f32 v[128:129], v[144:145], v[180:181]\n"
    "v_pk_mul_f32 v[136:137], v[152:153], v[180:181]\n"
    "v_pk_fma_f32 v[128:129], v[152:153], v[188:189], v[128:129] neg_lo:[1,0,0] neg_hi:[1,0,0]\n"
    "v_pk_fma_f32 v[136:137], v[144:145], v[188:189], v[136:137]\n"
    "v_pk_mul_f32 v[130:131], v[146:147], v[182:183]\n"
    "v_pk_mul_f32 v[138:139], v[154:155], v[182:183]\n"
    "v_pk_fma_f32 v[130:131], v[154:155], v[190:191], v[130:131] neg_lo:[1,0,0] neg_hi:[1,0,0]\n"
    "v_pk_fma_f32 v[138:139], v[146:147], v[190:191], v[138:139]\n"
    "v_pk_mul_f32 v[132:133], v[148:149], v[184:185]\n"
    "v_pk_mul_f32 v[140:141], v[156:157], v[184:185]\n"
    "v_pk_fma_f32 v[132:133], v[156:157], v[192:193], v[132:133] neg_lo:[1,0,0] neg_hi:[1,0,0]\n"
    "v_pk_fma_f32 v[140:141], v[148:149], v[192:193], v[140:141]\n"
    "s_add_u32 s68, s68, 4096\n"
    "buffer_load_dwordx4 v[80:83], v11, s[32:35], s68 offen sc1\n"
    "v_pk_mul_f32 v[134:135], v[150:151], v[186:187]\n"
    "v_pk_mul_f32 v[142:143], v[158:159], v[186:187]\n"
    "v_pk_fma_f32 v[134:135], v[158:159], v[194:195], v[134:135] neg_lo:[1,0,0] neg_hi:[1,0,0]\n"
    "v_pk_fma_f32 v[142:143], v[150:151], v[194:195], v[142:143]\n"
    "v_cvt_pk_bf16_f32 v168, v128, v129\n"
    "v_cvt_pk_bf16_f32 v169, v130, v131\n"
    "v_cvt_pk_bf16_f32 v170, v132, v133\n"
    "v_cvt_pk_bf16_f32 v171, v134, v135\n"
    "v_cvt_pk_bf16_f32 v172, v136, v137\n"
    "v_cvt_pk_bf16_f32 v173, v138, v139\n"
    "v_cvt_pk_bf16_f32 v174, v140, v141\n"
    "buffer_load_dwordx4 v[84:87], v12, s[32:35], s68 offen sc1\n"
    "v_cvt_pk_bf16_f32 v175, v142, v143\n"
    "v_cvt_pk_bf16_f32 v176, -v136, -v137\n"
    "v_cvt_pk_bf16_f32 v177, -v138, -v139\n"
    "v_cvt_pk_bf16_f32 v178, -v140, -v141\n"
    "v_cvt_pk_bf16_f32 v179, -v142, -v143\n"
    "v_mfma_f32_16x16x32_bf16 v[144:147], a[224:227], v[168:171], 0\n"
    "v_mfma_f32_16x16x32_bf16 v[152:155], a[224:227], v[172:175], 0\n"
    "buffer_load_dwordx4 v[88:91], v11, s[32:35], s68 offen offset:2048 sc1\n"
    "v_mfma_f32_16x16x32_bf16 v[148:151], a[232:235], v[168:171], 0\n"
    "v_mfma_f32_16x16x32_bf16 v[156:159], a[232:235], v[172:175], 0\n"
    "v_mfma_f32_16x16x32_bf16 v[144:147], a[228:231], v[176:179], v[144:147]\n"
    "v_mfma_f32_16x16x32_bf16 v[152:155], a[228:231], v[168:171], v[152:155]\n"
    "buffer_load_dwordx4 v[92:95], v12, s[32:35], s68 offen offset:2048 sc1\n"
    "v_mfma_f32_16x16x32_bf16 v[148:151], a[236:239], v[176:179], v[148:151]\n"
    "v_mfma_f32_16x16x32_bf16 v[156:159], a[236:239], v[168:171], v[156:159]\n"
    "ds_read_b128 v[180:183], v13 offset:9216\n"
    "ds_read_b128 v[184:187], v13 offset:10240\n"
    "ds_read_b128 v[188:191], v13 offset:11264\n"
    "ds_read_b128 v[192:195], v13 offset:12288\n"
    "ds_read_b128 v[196:199], v13 offset:13312\n"
    "s_add_u32 s68, s68, 4096\n"
    "buffer_load_dwordx4 v[96:99], v11, s[32:35], s68 offen sc1\n"
    "ds_read_b128 v[200:203], v13 offset:14336\n"
    "ds_read_b128 v[204:207], v13 offset:15360\n"
    "ds_read_b128 a[240:243], v13 offset:4096\n"
    "ds_read_b128 a[244:247], v13 offset:5120\n"
    "ds_read_b128 a[248:251], v13 offset:6144\n"
    "ds_read_b128 a[252:255], v13 offset:7168\n"
    "v_pk_mul_f32 v[128:129], v[144:145], v[224:225]\n"
    "v_pk_mul_f32 v[136:137], v[152:153], v[224:225]\n"
    "v_pk_fma_f32 v[128:129], v[152:153], v[232:233], v[128:129] neg_lo:[1,0,0] neg_hi:[1,0,0]\n"
    "v_pk_fma_f32 v[136:137], v[144:145], v[232:233], v[136:137]\n"
    "v_pk_mul_f32 v[130:131], v[146:147], v[226:227]\n"
    "v_pk_mul_f32 v[138:139], v[154:155], v[226:227]\n"
    "buffer_load_dwordx4 v[100:103], v12, s[32:35], s68 offen sc1\n"
    "v_pk_fma_f32 v[130:131], v[154:155], v[234:235], v[130:131] neg_lo:[1,0,0] neg_hi:[1,0,0]\n"
    "v_pk_fma_f32 v[138:139], v[146:147], v[234:235], v[138:139]\n"
    "v_pk_mul_f32 v[132:133], v[148:149], v[228:229]\n"
    "v_pk_mul_f32 v[140:141], v[156:157], v[228:229]\n"
    "v_pk_fma_f32 v[132:133], v[156:157], v[236:237], v[132:133] neg_lo:[1,0,0] neg_hi:[1,0,0]\n"
    "v_pk_fma_f32 v[140:141], v[148:149], v[236:237], v[140:141]\n"
    "v_pk_mul_f32 v[134:135], v[150:151], v[230:231]\n"
    "v_pk_mul_f32 v[142:143], v[158:159], v[230:231]\n"
    "v_pk_fma_f32 v[134:135], v[158:159], v[238:239], v[134:135] neg_lo:[1,0,0] neg_hi:[1,0,0]\n"
    "v_pk_fma_f32 v[142:143], v[150:151], v[238:239], v[142:143]\n"
    "v_cvt_pk_bf16_f32 v144, v128, v129\n"
    "v_cvt_pk_bf16_f32 v145, v130, v131\n"
    "buffer_load_dwordx4 v[104:107], v11, s[32:35], s68 offen offset:2048 sc1\n"
    "v_cvt_pk_bf16_f32 v146, v132, v133\n"
    "v_cvt_pk_bf16_f32 v147, v134, v135\n"
    "v_cvt_pk_bf16_f32 v148, v136, v137\n"
    "v_cvt_pk_bf16_f32 v149, v138, v139\n"
    "v_cvt_pk_bf16_f32 v150, v140, v141\n"
    "v_cvt_pk_bf16_f32 v151, v142, v143\n"
    "ds_write_b64 v243, v[144:145] offset:0\n"
    "ds_write_b64 v243, v[146:147] offset:32\n"
    "ds_write_b64 v243, v[148:149] offset:4608\n"
    "ds_write_b64 v243, v[150:151] offset:4640\n"
    "ds_read_b128 a[224:227], v13 offset:0\n"
    "ds_read_b128 a[228:231], v13 offset:1024\n"
    "ds_read_b128 a[232:235], v13 offset:2048\n"
    "buffer_load_dwordx4 v[108:111], v12, s[32:35], s68 offen offset:2048 sc1\n"
    "ds_read_b128 a[236:239], v13 offset:3072\n"
    "ds_read_b128 v[176:179], v13 offset:8192\n"
    "s_waitcnt vmcnt(16) lgkmcnt(0)\n"
    "s_barrier\n"
    "s_add_u32 s3, s3, 32768\n"
    "s_cmp_lg_u32 s44, 0\n"
    "s_cbranch_scc1 NP16_%=\n"
    "s_mov_b64 exec, 1\n"
    "buffer_store_dwordx2 v[222:223], off, s[32:35], s3 sc1\n"
    "s_mov_b64 exec, -1\n"
    "NP16_%=:\n"
    "ds_read_b64_tr_b16 v[128:129], v244 offset:0\n"
    "ds_read_b64_tr_b16 v[130:131], v244 offset:576\n"
    "ds_read_b64_tr_b16 v[132:133], v244 offset:2304\n"
    "ds_read_b64_tr_b16 v[134:135], v244 offset:2880\n"
    "ds_read_b64_tr_b16 v[136:137], v245 offset:0\n"
    "ds_read_b64_tr_b16 v[138:139], v245 offset:576\n"
    "ds_read_b64_tr_b16 v[140:141], v245 offset:2304\n"
    "ds_read_b64_tr_b16 v[142:143], v245 offset:2880\n"
    "v_and_b32 v208, 15, v0\n"
    "s_add_u32 s68, s68, 4096\n"
    "buffer_load_dwordx4 v[112:115], v11, s[32:35], s68 offen sc1\n"
    "v_mul_u32_u24 v208, 144, v208\n"
    "v_lshrrev_b32 v209, 4, v0\n"
    "v_lshl_add_u32 v208, v209, 4, v208\n"
    "s_mul_i32 s59, s44, 2304\n"
    "s_add_u32 s59, s59, 104448\n"
    "v_add_u32 v208, s59, v208\n"
    "v_lshrrev_b32 v209, 3, v0\n"
    "v_mul_u32_u24 v209, 144, v209\n"
    "v_and_b32 v210, 7, v0\n"
    "s_waitcnt lgkmcnt(6)\n"
    "v_mfma_f32_16x16x32_bf16 v[144:147], a[224:227], v[128:131], 0\n"
    "buffer_load_dwordx4 v[116:119], v12, s[32:35], s68 offen sc1\n"
    "s_waitcnt lgkmcnt(3)\n"
    "v_xor_b32 v136, v246, v136\n"
    "v_mfma_f32_16x16x32_bf16 v[148:151], a[240:243], v[128:131], 0\n"
    "v_xor_b32 v137, v246, v137\n"
    "v_mfma_f32_16x16x32_bf16 v[152:155], v[176:179], v[128:131], 0\n"
    "s_waitcnt lgkmcnt(2)\n"
    "v_xor_b32 v138, v246, v138\n"
    "v_mfma_f32_16x16x32_bf16 v[156:159], v[192:195], v[128:131], 0\n"
    "buffer_load_dwordx4 v[120:123], v11, s[32:35], s68 offen offset:2048 sc1\n"
    "v_xor_b32 v139, v246, v139\n"
    "v_mfma_f32_16x16x32_bf16 v[144:147], a[232:235], v[132:135], v[144:147]\n"
    "s_waitcnt lgkmcnt(1)\n"
    "v_xor_b32 v140, v246, v140\n"
    "v_mfma_f32_16x16x32_bf16 v[148:151], a[248:251], v[132:135], v[148:151]\n"
    "v_xor_b32 v141, v246, v141\n"
    "v_mfma_f32_16x16x32_bf16 v[152:155], v[184:187], v[132:135], v[152:155]\n"
    "buffer_load_dwordx4 v[124:127], v12, s[32:35], s68 offen offset:2048 sc1\n"
    "s_waitcnt lgkmcnt(0)\n"
    "v_xor_b32 v142, v246, v142\n"
    "v_mfma_f32_16x16x32_bf16 v[156:159], v[200:203], v[132:135], v[156:159]\n"
    "v_xor_b32 v143, v246, v143\n"
    "v_mfma_f32_16x16x32_bf16 v[144:147], a[228:231], v[136:139], v[144:147]\n"
    "v_lshl_add_u32 v209, v210, 4, v209\n"
    "v_mfma_f32_16x16x32_bf16 v[148:151], a[244:247], v[136:139], v[148:151]\n"
    "buffer_load_dwordx4 a[196:199], v3, s[32:35], s67 offen sc1\n"
    "v_add_u32 v209, s59, v209\n"
    "v_mfma_f32_16x16x32_bf16 v[152:155], v[180:183], v[136:139], v[152:155]\n"
    "s_or_b32 s59, s75, 128\n"
    "v_mfma_f32_16x16x32_bf16 v[156:159], v[196:199], v[136:139], v[156:159]\n"
    "s_add_u32 s59, s59, s79\n"
    "v_mfma_f32_16x16x32_bf16 v[144:147], a[236:239], v[140:143], v[144:147]\n"
    "buffer_load_dwordx4 a[200:203], v2, s[32:35], s67 offen offset:2048 sc1\n"
    "v_mfma_f32_16x16x32_bf16 v[148:151], a[252:255], v[140:143], v[148:151]\n"
    "v_mfma_f32_16x16x32_bf16 v[152:155], v[188:191], v[140:143], v[152:155]\n"
    "v_mfma_f32_16x16x32_bf16 v[156:159], v[204:207], v[140:143], v[156:159]\n"
    "s_nop 3\n"
    "v_cvt_pk_bf16_f32 v160, v144, v145\n"
    "v_cvt_pk_bf16_f32 v161, v146, v147\n"
    "v_cvt_pk_bf16_f32 v162, v148, v149\n"
    "v_cvt_pk_bf16_f32 v163, v150, v151\n"
    "ds_write_b128 v208, v[160:163] offset:0\n"
    "v_cvt_pk_bf16_f32 v164, v152, v153\n"
    "v_cvt_pk_bf16_f32 v165, v154, v155\n"
    "v_cvt_pk_bf16_f32 v166, v156, v157\n"
    "v_cvt_pk_bf16_f32 v167, v158, v159\n"
    "ds_write_b128 v208, v[164:167] offset:64\n"
    "ds_read_b128 v[128:131], v209 offset:0\n"
    "ds_read_b128 v[132:135], v209 offset:1152\n"
    "s_waitcnt lgkmcnt(1)\n"
    "buffer_store_dwordx4 v[128:131], v247, s[32:35], s79 offen sc1\n"
    "s_waitcnt lgkmcnt(0)\n"
    "buffer_store_dwordx4 v[132:135], v247, s[32:35], s59 offen sc1\n"
    "; ---- down projection ----\n"
    "buffer_load_dwordx4 a[204:207], v3, s[32:35], s67 offen offset:2048 sc1\n"
    "v_add_u32 v221, 32768, v221\n"
    "v_mfma_f32_32x32x16_bf16 v[128:143], a[0:3], v[16:19], 0\n"
    "v_lshrrev_b32 v192, 5, v0\n"
    "v_lshlrev_b32 v193, 6, v192\n"
    "v_xor_b32 v4, v193, v1\n"
    "v_mfma_f32_32x32x16_bf16 v[144:159], a[0:3], v[20:23], 0\n"
    "s_add_u32 s67, s67, 4096\n"
    "buffer_load_dwordx4 a[208:211], v2, s[32:35], s67 offen sc1\n"
    "v_and_b32 v193, 31, v0\n"
    "v_lshl_add_u32 v193, s58, 5, v193\n"
    "v_mul_u32_u24 v10, 0x7000, v193\n"
    "v_mfma_f32_32x32x16_bf16 v[160:175], a[4:7], v[16:19], 0\n"
    "v_lshl_add_u32 v10, v192, 5, v10\n"
    "v_lshrrev_b32 v210, 2, v0\n"
    "v_lshl_add_u32 v210, s58, 5, v210\n"
    "v_mfma_f32_32x32x16_bf16 v[176:191], a[4:7], v[20:23], 0\n"
    "buffer_load_dwordx4 a[212:215], v3, s[32:35], s67 offen sc1\n"
    "v_mul_u32_u24 v210, 0x7000, v210\n"
    "v_and_b32 v211, 3, v0\n"
    "v_lshl_add_u32 v210, v211, 4, v210\n"
    "buffer_load_dwordx2 v[240:241], v221, s[32:35], 0 offen sc1\n"
    "v_mfma_f32_32x32x16_bf16 v[128:143], a[8:11], v[24:27], v[128:143]\n"
    "s_lshl_b32 s48, s44, 11\n"
    "v_and_b32 v211, 31, v0\n"
    "v_lshl_add_u32 v211, v211, 1, v192\n"
    "v_mfma_f32_32x32x16_bf16 v[144:159], a[8:11], v[28:31], v[144:159]\n"
    "buffer_load_dwordx4 a[216:219], v2, s[32:35], s67 offen offset:2048 sc1\n"
    "v_lshl_add_u32 v211, v211, 5, s48\n"
    "v_add_u32 v211, 135168, v211\n"
    "v_lshlrev_b32 v5, 11, v192\n"
    "v_mfma_f32_32x32x16_bf16 v[160:175], a[12:15], v[24:27], v[160:175]\n"
    "v_bfe_u32 v192, v0, 3, 1\n"
    "v_lshl_add_u32 v5, v192, 9, v5\n"
    "v_xor_b32 v192, s58, v192\n"
    "v_mfma_f32_32x32x16_bf16 v[176:191], a[12:15], v[28:31], v[176:191]\n"
    "buffer_load_dwordx4 a[220:223], v3, s[32:35], s67 offen offset:2048 sc1\n"
    "v_lshl_add_u32 v5, v192, 6, v5\n"
    "v_bfe_u32 v192, v0, 2, 1\n"
    "v_lshl_add_u32 v5, v192, 7, v5\n"
    "v_mfma_f32_32x32x16_bf16 v[128:143], a[16:19], v[32:35], v[128:143]\n"
    "v_and_b32 v192, 3, v0\n"
    "v_lshl_add_u32 v5, v192, 4, v5\n"
    "v_bfe_u32 v192, v0, 4, 1\n"
    "v_mfma_f32_32x32x16_bf16 v[144:159], a[16:19], v[36:39], v[144:159]\n"
    "s_add_u32 m0, s71, 57344\n"
    "s_add_u32 s51, s47, 401408\n"
    "buffer_load_dwordx4 v1, s[36:39], s51 offen lds\n"
    "v_lshl_add_u32 v5, v192, 3, v5\n"
    "s_lshr_b32 s59, s44, 1\n"
    "v_lshl_add_u32 v5, s59, 8, v5\n"
    "v_mfma_f32_32x32x16_bf16 v[160:175], a[20:23], v[32:35], v[160:175]\n"
    "s_mul_i32 s46, s59, 448\n"
    "s_lshl_b32 s76, s59, 31\n"
    "s_or_b32 s76, s76, 1.0\n"
    "v_mfma_f32_32x32x16_bf16 v[176:191], a[20:23], v[36:39], v[176:191]\n"
    "s_add_u32 m0, s71, 61440\n"
    "s_add_u32 s51, s47, 405504\n"
    "buffer_load_dwordx4 v1, s[36:39], s51 offen lds\n"
    "s_mul_i32 s59, s59, 14336\n"
    "s_add_u32 s59, s59, 57344\n"
    "v_add_u32 v6, s59, v1\n"
    "v_mfma_f32_32x32x16_bf16 v[128:143], a[24:27], v[40:43], v[128:143]\n"
    "s_mul_i32 s59, s55, 896\n"
    "s_add_u32 s46, s46, s59\n"
    "s_lshl_b32 s59, s56, 6\n"
    "v_mfma_f32_32x32x16_bf16 v[144:159], a[24:27], v[44:47], v[144:159]\n"
    "s_add_u32 m0, s71, 65536\n"
    "s_add_u32 s51, s47, 409600\n"
    "buffer_load_dwordx4 v1, s[36:39], s51 offen lds\n"
    "s_add_u32 s46, s46, s59\n"
    "s_xor_b32 s59, s44, 1\n"
    "s_mul_i32 s59, s59, 12288\n"
    "v_mfma_f32_32x32x16_bf16 v[160:175], a[28:31], v[40:43], v[160:175]\n"
    "s_add_u32 s59, s59, 86016\n"
    "v_add_u32 v7, s59, v1\n"
    "s_xor_b32 s59, s44, 2\n"
    "v_mfma_f32_32x32x16_bf16 v[176:191], a[28:31], v[44:47], v[176:191]\n"
    "s_add_u32 m0, s71, 69632\n"
    "s_add_u32 s51, s47, 413696\n"
    "buffer_load_dwordx4 v1, s[36:39], s51 offen lds\n"
    "s_mul_i32 s59, s59, 12288\n"
    "s_add_u32 s59, s59, 90112\n"
    "v_add_u32 v8, s59, v1\n"
    "s_waitcnt vmcnt(10)\n"
    "s_barrier\n"
    "s_cmp_lg_u32 s44, 0\n"
    "s_cbranch_scc1 NP17_%=\n"
    "s_mov_b64 exec, 1\n"
    "buffer_store_dwordx2 v[222:223], off, s[32:35], s22 sc1\n"
    "s_mov_b64 exec, -1\n"
    "NP17_%=:\n"
    "v_mfma_f32_32x32x16_bf16 v[128:143], a[32:35], v[48:51], v[128:143]\n"
    "s_xor_b32 s59, s44, 3\n"
    "s_mul_i32 s59, s59, 12288\n"
    "s_add_u32 s59, s59, 94208\n"
    "v_mfma_f32_32x32x16_bf16 v[144:159], a[32:35], v[52:55], v[144:159]\n"
    "s_add_u32 m0, s71, 73728\n"
    "s_add_u32 s51, s47, 417792\n"
    "buffer_load_dwordx4 v1, s[36:39], s51 offen lds\n"
    "v_add_u32 v9, s59, v1\n"
    "v_add_u32 v208, 131072, v1\n"
    "v_min_u32 v209, 768, v1\n"
    "v_mfma_f32_32x32x16_bf16 v[160:175], a[36:39], v[48:51], v[160:175]\n"
    "v_add_u32 v209, 132096, v209\n"
    "s_mul_i32 s45, s44, 14336\n"
    "s_mul_i32 s52, s55, 57344\n"
    "v_mfma_f32_32x32x16_bf16 v[176:191], a[36:39], v[52:55], v[176:191]\n"
    "s_add_u32 m0, s71, 77824\n"
    "s_add_u32 s51, s47, 421888\n"
    "buffer_load_dwordx4 v1, s[36:39], s51 offen lds\n"
    "s_add_u32 s52, s52, s45\n"
    "s_add_u32 s52, s52, 2097152\n"
    "s_mul_i32 s59, s44, 12288\n"
    "v_mfma_f32_32x32x16_bf16 v[128:143], a[40:43], v[56:59], v[128:143]\n"
    "s_add_u32 s59, s59, 86016\n"
    "v_add_u32 v13, s59, v1\n"
    "s_xor_b32 s59, s44, 2\n"
    "v_mfma_f32_32x32x16_bf16 v[144:159], a[40:43], v[60:63], v[144:159]\n"
    "s_add_u32 m0, s71, 81920\n"
    "s_add_u32 s51, s47, 425984\n"
    "buffer_load_dwordx4 v1, s[36:39], s51 offen lds\n"
    "s_lshl_b32 s59, s59, 12\n"
    "s_add_u32 s59, s59, 147456\n"
    "v_add_u32 v14, s59, v1\n"
    "v_mfma_f32_32x32x16_bf16 v[160:175], a[44:47], v[56:59], v[160:175]\n"
    "v_xor_b32 v15, 0x2000, v14\n"
    "s_mov_b32 s0, 0xbfb8aa3b\n"
    "v_mfma_f32_32x32x16_bf16 v[176:191], a[44:47], v[60:63], v[176:191]\n"
    "v_mfma_f32_32x32x16_bf16 v[128:143], a[48:51], v[64:67], v[128:143]\n"
    "v_mfma_f32_32x32x16_bf16 v[144:159], a[48:51], v[68:71], v[144:159]\n"
    "v_mfma_f32_32x32x16_bf16 v[160:175], a[52:55], v[64:67], v[160:175]\n"
    "v_mfma_f32_32x32x16_bf16 v[176:191], a[52:55], v[68:71], v[176:191]\n"
    "s_waitcnt vmcnt(9)\n"
    "v_cmp_ne_u64 vcc, s[60:61], v[240:241]\n"
    "s_nop 1\n"
    "s_cmp_lg_u64 vcc, 0\n"
    "s_cbranch_scc1 SPIN18_%=\n"
    "BACK20_%=:\n"
    "v_mfma_f32_32x32x16_bf16 v[128:143], a[56:59], v[72:75], v[128:143]\n"
    "v_mfma_f32_32x32x16_bf16 v[144:159], a[56:59], v[76:79], v[144:159]\n"
    "s_add_u32 s68, s68, 4096\n"
    "buffer_load_dwordx4 v[16:19], v11, s[32:35], s68 offen sc1\n"
    "v_mfma_f32_32x32x16_bf16 v[160:175], a[60:63], v[72:75], v[160:175]\n"
    "v_mfma_f32_32x32x16_bf16 v[176:191], a[60:63], v[76:79], v[176:191]\n"
    "buffer_load_dwordx4 v[20:23], v12, s[32:35], s68 offen sc1\n"
    "v_mfma_f32_32x32x16_bf16 v[128:143], a[64:67], v[80:83], v[128:143]\n"
    "v_mfma_f32_32x32x16_bf16 v[144:159], a[64:67], v[84:87], v[144:159]\n"
    "buffer_load_dwordx4 v[24:27], v11, s[32:35], s68 offen offset:2048 sc1\n"
    "v_mfma_f32_32x32x16_bf16 v[160:175], a[68:71], v[80:83], v[160:175]\n"
    "v_mfma_f32_32x32x16_bf16 v[176:191], a[68:71], v[84:87], v[176:191]\n"
    "buffer_load_dwordx4 v[28:31], v12, s[32:35], s68 offen offset:2048 sc1\n"
    "v_mfma_f32_32x32x16_bf16 v[128:143], a[72:75], v[88:91], v[128:143]\n"
    "v_mfma_f32_32x32x16_bf16 v[144:159], a[72:75], v[92:95], v[144:159]\n"
    "s_add_u32 s68, s68, 4096\n"
    "buffer_load_dwordx4 v[32:35], v11, s[32:35], s68 offen sc1\n"
    "v_mfma_f32_32x32x16_bf16 v[160:175], a[76:79], v[88:91], v[160:175]\n"
    "v_mfma_f32_32x32x16_bf16 v[176:191], a[76:79], v[92:95], v[176:191]\n"
    "buffer_load_dwordx4 v[36:39], v12, s[32:35], s68 offen sc1\n"
    "v_mfma_f32_32x32x16_bf16 v[128:143], a[80:83], v[96:99], v[128:143]\n"
    "v_mfma_f32_32x32x16_bf16 v[144:159], a[80:83], v[100:103], v[144:159]\n"
    "buffer_load_dwordx4 v[40:43], v11, s[32:35], s68 offen offset:2048 sc1\n"
    "v_mfma_f32_32x32x16_bf16 v[160:175], a[84:87], v[96:99], v[160:175]\n"
    "v_mfma_f32_32x32x16_bf16 v[176:191], a[84:87], v[100:103], v[176:191]\n"
    "buffer_load_dwordx4 v[44:47], v12, s[32:35], s68 offen offset:2048 sc1\n"
    "v_mfma_f32_32x32x16_bf16 v[128:143], a[88:91], v[104:107], v[128:143]\n"
    "v_mfma_f32_32x32x16_bf16 v[144:159], a[88:91], v[108:111], v[144:159]\n"
    "s_add_u32 s68, s68, 4096\n"
    "buffer_load_dwordx4 v[48:51], v11, s[32:35], s68 offen sc1\n"
    "v_mfma_f32_32x32x16_bf16 v[160:175], a[92:95], v[104:107], v[160:175]\n"
    "v_mfma_f32_32x32x16_bf16 v[176:191], a[92:95], v[108:111], v[176:191]\n"
    "buffer_load_dwordx4 v[52:55], v12, s[32:35], s68 offen sc1\n"
    "v_mfma_f32_32x32x16_bf16 v[128:143], a[96:99], v[112:115], v[128:143]\n"
    "v_mfma_f32_32x32x16_bf16 v[144:159], a[96:99], v[116:119], v[144:159]\n"
    "buffer_load_dwordx4 v[56:59], v11, s[32:35], s68 offen offset:2048 sc1\n"
    "v_mfma_f32_32x32x16_bf16 v[160:175], a[100:103], v[112:115], v[160:175]\n"
    "v_mfma_f32_32x32x16_bf16 v[176:191], a[100:103], v[116:119], v[176:191]\n"
    "buffer_load_dwordx4 v[60:63], v12, s[32:35], s68 offen offset:2048 sc1\n"
    "v_mfma_f32_32x32x16_bf16 v[128:143], a[104:107], v[120:123], v[128:143]\n"
    "v_mfma_f32_32x32x16_bf16 v[144:159], a[104:107], v[124:127], v[144:159]\n"
    "s_add_u32 s68, s68, 4096\n"
    "buffer_load_dwordx4 v[64:67], v11, s[32:35], s68 offen sc1\n"
    "v_mfma_f32_32x32x16_bf16 v[160:175], a[108:111], v[120:123], v[160:175]\n"
    "v_mfma_f32_32x32x16_bf16 v[176:191], a[108:111], v[124:127], v[176:191]\n"
    "buffer_load_dwordx4 v[68:71], v12, s[32:35], s68 offen sc1\n"
    "s_waitcnt vmcnt(13)\n"
    "v_mfma_f32_32x32x16_bf16 v[128:143], a[112:115], v[16:19], v[128:143]\n"
    "s_waitcnt vmcnt(12)\n"
    "v_mfma_f32_32x32x16_bf16 v[144:159], a[112:115], v[20:23], v[144:159]\n"
    "buffer_load_dwordx4 v[72:75], v11, s[32:35], s68 offen offset:2048 sc1\n"
    "v_mfma_f32_32x32x16_bf16 v[160:175], a[116:119], v[16:19], v[160:175]\n"
    "v_mfma_f32_32x32x16_bf16 v[176:191], a[116:119], v[20:23], v[176:191]\n"
    "buffer_load_dwordx4 v[76:79], v12, s[32:35], s68 offen offset:2048 sc1\n"
    "s_waitcnt vmcnt(13)\n"
    "v_mfma_f32_32x32x16_bf16 v[128:143], a[120:123], v[24:27], v[128:143]\n"
    "s_waitcnt vmcnt(12)\n"
    "v_mfma_f32_32x32x16_bf16 v[144:159], a[120:123], v[28:31], v[144:159]\n"
    "s_add_u32 s68, s68, 4096\n"
    "buffer_load_dwordx4 v[80:83], v11, s[32:35], s68 offen sc1\n"
    "v_mfma_f32_32x32x16_bf16 v[160:175], a[124:127], v[24:27], v[160:175]\n"
    "v_mfma_f32_32x32x16_bf16 v[176:191], a[124:127], v[28:31], v[176:191]\n"
    "buffer_load_dwordx4 v[84:87], v12, s[32:35], s68 offen sc1\n"
    "s_cmp_lg_u32 s44, 0\n"
    "s_cbranch_scc1 NQ21_%=\n"
    "buffer_load_dwordx4 v[240:243], v208, s[32:35], 0 offen sc1\n"
    "buffer_load_dwordx4 v[244:247], v209, s[32:35], 0 offen sc1\n"
    "NQ21_%=:\n"
    "s_waitcnt vmcnt(13)\n"
    "v_mfma_f32_32x32x16_bf16 v[128:143], a[128:131], v[32:35], v[128:143]\n"
    "s_waitcnt vmcnt(12)\n"
    "v_mfma_f32_32x32x16_bf16 v[144:159], a[128:131], v[36:39], v[144:159]\n"
    "buffer_load_dwordx4 v[88:91], v11, s[32:35], s68 offen offset:2048 sc1\n"
    "v_mfma_f32_32x32x16_bf16 v[160:175], a[132:135], v[32:35], v[160:175]\n"
    "v_mfma_f32_32x32x16_bf16 v[176:191], a[132:135], v[36:39], v[176:191]\n"
    "buffer_load_dwordx4 v[92:95], v12, s[32:35], s68 offen offset:2048 sc1\n"
    "s_waitcnt vmcnt(13)\n"
    "v_mfma_f32_32x32x16_bf16 v[128:143], a[136:139], v[40:43], v[128:143]\n"
    "s_waitcnt vmcnt(12)\n"
    "v_mfma_f32_32x32x16_bf16 v[144:159], a[136:139], v[44:47], v[144:159]\n"
    "s_add_u32 s68, s68, 4096\n"
    "buffer_load_dwordx4 v[96:99], v11, s[32:35], s68 offen sc1\n"
    "v_mfma_f32_32x32x16_bf16 v[160:175], a[140:143], v[40:43], v[160:175]\n"
    "v_mfma_f32_32x32x16_bf16 v[176:191], a[140:143], v[44:47], v[176:191]\n"
    "buffer_load_dwordx4 v[100:103], v12, s[32:35], s68 offen sc1\n"
    "s_waitcnt vmcnt(13)\n"
    "v_mfma_f32_32x32x16_bf16 v[128:143], a[144:147], v[48:51], v[128:143]\n"
    "s_waitcnt vmcnt(12)\n"
    "v_mfma_f32_32x32x16_bf16 v[144:159], a[144:147], v[52:55], v[144:159]\n"
    "buffer_load_dwordx4 v[104:107], v11, s[32:35], s68 offen offset:2048 sc1\n"
    "v_mfma_f32_32x32x16_bf16 v[160:175], a[148:151], v[48:51], v[160:175]\n"
    "v_mfma_f32_32x32x16_bf16 v[176:191], a[148:151], v[52:55], v[176:191]\n"
    "buffer_load_dwordx4 v[108:111], v12, s[32:35], s68 offen offset:2048 sc1\n"
    "s_waitcnt vmcnt(13)\n"
    "v_mfma_f32_32x32x16_bf16 v[128:143], a[152:155], v[56:59], v[128:143]\n"
    "s_waitcnt vmcnt(12)\n"
    "v_mfma_f32_32x32x16_bf16 v[144:159], a[152:155], v[60:63], v[144:159]\n"
    "s_add_u32 s68, s68, 4096\n"
    "buffer_load_dwordx4 v[112:115], v11, s[32:35], s68 offen sc1\n"
    "v_mfma_f32_32x32x16_bf16 v[160:175], a[156:159], v[56:59], v[160:175]\n"
    "v_mfma_f32_32x32x16_bf16 v[176:191], a[156:159], v[60:63], v[176:191]\n"
    "buffer_load_dwordx4 v[116:119], v12, s[32:35], s68 offen sc1\n"
    "s_waitcnt vmcnt(13)\n"
    "v_mfma_f32_32x32x16_bf16 v[128:143], a[160:163], v[64:67], v[128:143]\n"
    "s_waitcnt vmcnt(12)\n"
    "v_mfma_f32_32x32x16_bf16 v[144:159], a[160:163], v[68:71], v[144:159]\n"
    "buffer_load_dwordx4 v[120:123], v11, s[32:35], s68 offen offset:2048 sc1\n"
    "v_mfma_f32_32x32x16_bf16 v[160:175], a[164:167], v[64:67], v[160:175]\n"
    "v_mfma_f32_32x32x16_bf16 v[176:191], a[164:167], v[68:71], v[176:191]\n"
    "buffer_load_dwordx4 v[124:127], v12, s[32:35], s68 offen offset:2048 sc1\n"
    "s_waitcnt vmcnt(13)\n"
    "v_mfma_f32_32x32x16_bf16 v[128:143], a[168:171], v[72:75], v[128:143]\n"
    "s_waitcnt vmcnt(12)\n"
    "v_mfma_f32_32x32x16_bf16 v[144:159], a[168:171], v[76:79], v[144:159]\n"
    "s_add_u32 m0, s48, 135168\n"
    "s_nop 0\n"
    "buffer_load_dwordx4 v210, s[24:27], s46 offen lds\n"
    "v_mfma_f32_32x32x16_bf16 v[160:175], a[172:175], v[72:75], v[160:175]\n"
    "v_mfma_f32_32x32x16_bf16 v[176:191], a[172:175], v[76:79], v[176:191]\n"
    "s_add_u32 m0, s48, 136192\n"
    "s_add_u32 s51, s46, 458752\n"
    "buffer_load_dwordx4 v210, s[24:27], s51 offen lds\n"
    "s_cmp_lg_u32 s44, 0\n"
    "s_cbranch_scc1 BACK24_%=\n"
    "s_waitcnt vmcnt(12)\n"
    "v_cmp_ne_u64 vcc, s[60:61], v[240:241]\n"
    "v_cmp_ne_u64 s[72:73], s[60:61], v[242:243]\n"
    "s_nop 1\n"
    "s_or_b64 vcc, vcc, s[72:73]\n"
    "v_cmp_ne_u64 s[72:73], s[60:61], v[244:245]\n"
    "s_nop 1\n"
    "s_or_b64 vcc, vcc, s[72:73]\n"
    "v_cmp_ne_u64 s[72:73], s[60:61], v[246:247]\n"
    "s_nop 1\n"
    "s_or_b64 vcc, vcc, s[72:73]\n"
    "s_nop 1\n"
    "s_cmp_lg_u64 vcc, 0\n"
    "s_cbranch_scc1 SPIN22_%=\n"
    "BACK24_%=:\n"
    "s_barrier\n"
    "s_waitcnt vmcnt(13)\n"
    "v_mfma_f32_32x32x16_bf16 v[128:143], a[176:179], v[80:83], v[128:143]\n"
    "s_waitcnt vmcnt(12)\n"
    "v_mfma_f32_32x32x16_bf16 v[144:159], a[176:179], v[84:87], v[144:159]\n"
    "s_add_u32 m0, s45, 0\n"
    "s_nop 0\n"
    "buffer_load_dwordx4 v4, s[32:35], s52 offen sc1 lds\n"
    "v_mfma_f32_32x32x16_bf16 v[160:175], a[180:183], v[80:83], v[160:175]\n"
    "v_mfma_f32_32x32x16_bf16 v[176:191], a[180:183], v[84:87], v[176:191]\n"
    "s_add_u32 m0, s45, 1024\n"
    "s_add_u32 s51, s52, 1024\n"
    "buffer_load_dwordx4 v4, s[32:35], s51 offen sc1 lds\n"
    "s_waitcnt vmcnt(13)\n"
    "v_mfma_f32_32x32x16_bf16 v[128:143], a[184:187], v[88:91], v[128:143]\n"
    "s_waitcnt vmcnt(12)\n"
    "v_mfma_f32_32x32x16_bf16 v[144:159], a[184:187], v[92:95], v[144:159]\n"
    "s_add_u32 m0, s45, 2048\n"
    "s_add_u32 s51, s52, 2048\n"
    "buffer_load_dwordx4 v4, s[32:35], s51 offen sc1 lds\n"
    "v_mfma_f32_32x32x16_bf16 v[160:175], a[188:191], v[88:91], v[160:175]\n"
    "v_mfma_f32_32x32x16_bf16 v[176:191], a[188:191], v[92:95], v[176:191]\n"
    "s_add_u32 m0, s45, 3072\n"
    "s_add_u32 s51, s52, 3072\n"
    "buffer_load_dwordx4 v4, s[32:35], s51 offen sc1 lds\n"
    "s_waitcnt vmcnt(13)\n"
    "v_mfma_f32_32x32x16_bf16 v[128:143], a[192:195], v[96:99], v[128:143]\n"
    "s_waitcnt vmcnt(12)\n"
    "v_mfma_f32_32x32x16_bf16 v[144:159], a[192:195], v[100:103], v[144:159]\n"
    "s_add_u32 m0, s45, 4096\n"
    "s_add_u32 s51, s52, 4096\n"
    "buffer_load_dwordx4 v4, s[32:35], s51 offen sc1 lds\n"
    "v_mfma_f32_32x32x16_bf16 v[160:175], a[196:199], v[96:99], v[160:175]\n"
    "v_mfma_f32_32x32x16_bf16 v[176:191], a[196:199], v[100:103], v[176:191]\n"
    "s_add_u32 m0, s45, 5120\n"
    "s_add_u32 s51, s52, 5120\n"
    "buffer_load_dwordx4 v4, s[32:35], s51 offen sc1 lds\n"
    "s_waitcnt vmcnt(13)\n"
    "v_mfma_f32_32x32x16_bf16 v[128:143], a[200:203], v[104:107], v[128:143]\n"
    "s_waitcnt vmcnt(12)\n"
    "v_mfma_f32_32x32x16_bf16 v[144:159], a[200:203], v[108:111], v[144:159]\n"
    "s_add_u32 m0, s45, 6144\n"
    "s_add_u32 s51, s52, 6144\n"
    "buffer_load_dwordx4 v4, s[32:35], s51 offen sc1 lds\n"
    "v_mfma_f32_32x32x16_bf16 v[160:175], a[204:207], v[104:107], v[160:175]\n"
    "v_mfma_f32_32x32x16_bf16 v[176:191], a[204:207], v[108:111], v[176:191]\n"
    "s_add_u32 m0, s45, 7168\n"
    "s_add_u32 s51, s52, 7168\n"
    "buffer_load_dwordx4 v4, s[32:35], s51 offen sc1 lds\n"
    "s_waitcnt vmcnt(13)\n"
    "v_mfma_f32_32x32x16_bf16 v[128:143], a[208:211], v[112:115], v[128:143]\n"
    "s_waitcnt vmcnt(12)\n"
    "v_mfma_f32_32x32x16_bf16 v[144:159], a[208:211], v[116:119], v[144:159]\n"
    "s_add_u32 m0, s45, 8192\n"
    "s_add_u32 s51, s52, 8192\n"
    "buffer_load_dwordx4 v4, s[32:35], s51 offen sc1 lds\n"
    "v_mfma_f32_32x32x16_bf16 v[160:175], a[212:215], v[112:115], v[160:175]\n"
    "v_mfma_f32_32x32x16_bf16 v[176:191], a[212:215], v[116:119], v[176:191]\n"
    "s_add_u32 m0, s45, 9216\n"
    "s_add_u32 s51, s52, 9216\n"
    "buffer_load_dwordx4 v4, s[32:35], s51 offen sc1 lds\n"
    "s_waitcnt vmcnt(13)\n"
    "v_mfma_f32_32x32x16_bf16 v[128:143], a[216:219], v[120:123], v[128:143]\n"
    "s_waitcnt vmcnt(12)\n"
    "v_mfma_f32_32x32x16_bf16 v[144:159], a[216:219], v[124:127], v[144:159]\n"
    "s_add_u32 m0, s45, 10240\n"
    "s_add_u32 s51, s52, 10240\n"
    "buffer_load_dwordx4 v4, s[32:35], s51 offen sc1 lds\n"
    "v_mfma_f32_32x32x16_bf16 v[160:175], a[220:223], v[120:123], v[160:175]\n"
    "v_mfma_f32_32x32x16_bf16 v[176:191], a[220:223], v[124:127], v[176:191]\n"
    "s_add_u32 m0, s45, 11264\n"
    "s_add_u32 s51, s52, 11264\n"
    "buffer_load_dwordx4 v4, s[32:35], s51 offen sc1 lds\n"
    "s_or_b32 s51, s2, s44\n"
    "s_cbranch_scc1 NE25_%=\n"
    "s_mov_b64 exec, 1\n"
    "buffer_store_dwordx2 v[222:223], off, s[32:35], 0 sc1\n"
    "s_mov_b64 exec, -1\n"
    "NE25_%=:\n"
    "s_add_u32 m0, s45, 12288\n"
    "s_add_u32 s51, s52, 12288\n"
    "buffer_load_dwordx4 v4, s[32:35], s51 offen sc1 lds\n"
    "ds_write_b128 v7, v[144:147] offset:0\n"
    "s_add_u32 m0, s45, 13312\n"
    "s_add_u32 s51, s52, 13312\n"
    "buffer_load_dwordx4 v4, s[32:35], s51 offen sc1 lds\n"
    "ds_write_b128 v7, v[148:151] offset:1024\n"
    "ds_write_b128 v7, v[152:155] offset:2048\n"
    "ds_write_b128 v7, v[156:159] offset:3072\n"
    "ds_write_b128 v8, v[160:163] offset:0\n"
    "ds_write_b128 v8, v[164:167] offset:1024\n"
    "ds_write_b128 v8, v[168:171] offset:2048\n"
    "ds_write_b128 v8, v[172:175] offset:3072\n"
    "ds_write_b128 v9, v[176:179] offset:0\n"
    "ds_write_b128 v9, v[180:183] offset:1024\n"
    "ds_write_b128 v9, v[184:187] offset:2048\n"
    "ds_write_b128 v9, v[188:191] offset:3072\n"
    "s_waitcnt vmcnt(0) lgkmcnt(0)\n"
    "s_barrier\n"
    "; ---- inverse DFT over d, with the partial sums added underneath ----\n"
    "ds_read_b128 a[224:227], v6 offset:0\n"
    "ds_read_b64_tr_b16 a[228:229], v5 offset:0\n"
    "ds_read_b64_tr_b16 a[230:231], v5 offset:1024\n"
    "ds_read_b128 a[232:235], v6 offset:1024\n"
    "ds_read_b64_tr_b16 a[236:237], v5 offset:4096\n"
    "ds_read_b64_tr_b16 a[238:239], v5 offset:5120\n"
    "ds_read_b128 a[240:243], v6 offset:2048\n"
    "ds_read_b64_tr_b16 a[244:245], v5 offset:8192\n"
    "ds_read_b64_tr_b16 a[246:247], v5 offset:9216\n"
    "s_waitcnt lgkmcnt(6)\n"
    "v_mfma_f32_32x32x16_bf16 v[192:207], a[224:227], a[228:231], 0\n"
    "ds_read_b128 a[248:251], v6 offset:3072\n"
    "ds_read_b64_tr_b16 a[252:253], v5 offset:12288\n"
    "ds_read_b64_tr_b16 a[254:255], v5 offset:13312\n"
    "ds_read_b128 v[16:19], v13 offset:0\n"
    "s_waitcnt lgkmcnt(7)\n"
    "v_mfma_f32_32x32x16_bf16 v[192:207], a[232:235], a[236:239], v[192:207]\n"
    "ds_read_b128 a[224:227], v6 offset:4096\n"
    "ds_read_b64_tr_b16 a[228:229], v5 offset:16384\n"
    "ds_read_b64_tr_b16 a[230:231], v5 offset:17408\n"
    "ds_read_b128 v[20:23], v13 offset:1024\n"
    "s_waitcnt lgkmcnt(8)\n"
    "v_mfma_f32_32x32x16_bf16 v[192:207], a[240:243], a[244:247], v[192:207]\n"
    "ds_read_b128 a[232:235], v6 offset:5120\n"
    "ds_read_b64_tr_b16 a[236:237], v5 offset:20480\n"
    "ds_read_b64_tr_b16 a[238:239], v5 offset:21504\n"
    "ds_read_b128 v[24:27], v13 offset:2048\n"
    "s_waitcnt lgkmcnt(9)\n"
    "v_mfma_f32_32x32x16_bf16 v[192:207], a[248:251], a[252:255], v[192:207]\n"
    "ds_read_b128 a[240:243], v6 offset:6144\n"
    "ds_read_b64_tr_b16 a[244:245], v5 offset:24576\n"
    "ds_read_b64_tr_b16 a[246:247], v5 offset:25600\n"
    "ds_read_b128 v[28:31], v13 offset:3072\n"
    "s_waitcnt lgkmcnt(12)\n"
    "v_add_f32 v128, v128, v16\n"
    "v_add_f32 v129, v129, v17\n"
    "v_add_f32 v130, v130, v18\n"
    "v_add_f32 v131, v131, v19\n"
    "s_waitcnt lgkmcnt(9)\n"
    "v_mfma_f32_32x32x16_bf16 v[192:207], a[224:227], a[228:231], v[192:207]\n"
    "ds_read_b128 a[248:251], v6 offset:7168\n"
    "ds_read_b64_tr_b16 a[252:253], v5 offset:28672\n"
    "ds_read_b64_tr_b16 a[254:255], v5 offset:29696\n"
    "ds_read_b128 v[32:35], v13 offset:4096\n"
    "s_waitcnt lgkmcnt(12)\n"
    "v_add_f32 v132, v132, v20\n"
    "v_add_f32 v133, v133, v21\n"
    "v_add_f32 v134, v134, v22\n"
    "v_add_f32 v135, v135, v23\n"
    "s_waitcnt lgkmcnt(9)\n"
    "v_mfma_f32_32x32x16_bf16 v[192:207], a[232:235], a[236:239], v[192:207]\n"
    "ds_read_b128 a[224:227], v6 offset:8192\n"
    "ds_read_b64_tr_b16 a[228:229], v5 offset:32768\n"
    "ds_read_b64_tr_b16 a[230:231], v5 offset:33792\n"
    "ds_read_b128 v[36:39], v13 offset:5120\n"
    "s_waitcnt lgkmcnt(12)\n"
    "v_add_f32 v136, v136, v24\n"
    "v_add_f32 v137, v137, v25\n"
    "v_add_f32 v138, v138, v26\n"
    "v_add_f32 v139, v139, v27\n"
    "s_waitcnt lgkmcnt(9)\n"
    "v_mfma_f32_32x32x16_bf16 v[192:207], a[240:243], a[244:247], v[192:207]\n"
    "ds_read_b128 a[232:235], v6 offset:9216\n"
    "ds_read_b64_tr_b16 a[236:237], v5 offset:36864\n"
    "ds_read_b64_tr_b16 a[238:239], v5 offset:37888\n"
    "ds_read_b128 v[40:43], v13 offset:6144\n"
    "s_waitcnt lgkmcnt(12)\n"
    "v_add_f32 v140, v140, v28\n"
    "v_add_f32 v141, v141, v29\n"
    "v_add_f32 v142, v142, v30\n"
    "v_add_f32 v143, v143, v31\n"
    "s_waitcnt lgkmcnt(9)\n"
    "v_mfma_f32_32x32x16_bf16 v[192:207], a[248:251], a[252:255], v[192:207]\n"
    "ds_read_b128 a[240:243], v6 offset:10240\n"
    "ds_read_b64_tr_b16 a[244:245], v5 offset:40960\n"
    "ds_read_b64_tr_b16 a[246:247], v5 offset:41984\n"
    "ds_read_b128 v[44:47], v13 offset:7168\n"
    "s_waitcnt lgkmcnt(12)\n"
    "v_add_f32 v128, v128, v32\n"
    "v_add_f32 v129, v129, v33\n"
    "v_add_f32 v130, v130, v34\n"
    "v_add_f32 v131, v131, v35\n"
    "s_waitcnt lgkmcnt(9)\n"
    "v_mfma_f32_32x32x16_bf16 v[192:207], a[224:227], a[228:231], v[192:207]\n"
    "ds_read_b128 a[248:251], v6 offset:11264\n"
    "ds_read_b64_tr_b16 a[252:253], v5 offset:45056\n"
    "ds_read_b64_tr_b16 a[254:255], v5 offset:46080\n"
    "ds_read_b128 v[48:51], v13 offset:8192\n"
    "s_waitcnt lgkmcnt(12)\n"
    "v_add_f32 v132, v132, v36\n"
    "v_add_f32 v133, v133, v37\n"
    "v_add_f32 v134, v134, v38\n"
    "v_add_f32 v135, v135, v39\n"
    "s_waitcnt lgkmcnt(9)\n"
    "v_mfma_f32_32x32x16_bf16 v[192:207], a[232:235], a[236:239], v[192:207]\n"
    "ds_read_b128 a[224:227], v6 offset:12288\n"
    "ds_read_b64_tr_b16 a[228:229], v5 offset:49152\n"
    "ds_read_b64_tr_b16 a[230:231], v5 offset:50176\n"
    "ds_read_b128 v[52:55], v13 offset:9216\n"
    "s_waitcnt lgkmcnt(12)\n"
    "v_add_f32 v136, v136, v40\n"
    "v_add_f32 v137, v137, v41\n"
    "v_add_f32 v138, v138, v42\n"
    "v_add_f32 v139, v139, v43\n"
    "s_waitcnt lgkmcnt(9)\n"
    "v_mfma_f32_32x32x16_bf16 v[192:207], a[240:243], a[244:247], v[192:207]\n"
    "ds_read_b128 a[232:235], v6 offset:13312\n"
    "ds_read_b64_tr_b16 a[236:237], v5 offset:53248\n"
    "ds_read_b64_tr_b16 a[238:239], v5 offset:54272\n"
    "ds_read_b128 v[56:59], v13 offset:10240\n"
    "s_waitcnt lgkmcnt(12)\n"
    "v_add_f32 v140, v140, v44\n"
    "v_add_f32 v141, v141, v45\n"
    "v_add_f32 v142, v142, v46\n"
    "v_add_f32 v143, v143, v47\n"
    "s_waitcnt lgkmcnt(9)\n"
    "v_mfma_f32_32x32x16_bf16 v[192:207], a[248:251], a[252:255], v[192:207]\n"
    "ds_read_b128 v[60:63], v13 offset:11264\n"
    "s_waitcnt lgkmcnt(9)\n"
    "v_add_f32 v128, v128, v48\n"
    "v_add_f32 v129, v129, v49\n"
    "v_add_f32 v130, v130, v50\n"
    "v_add_f32 v131, v131, v51\n"
    "s_waitcnt lgkmcnt(6)\n"
    "v_mfma_f32_32x32x16_bf16 v[192:207], a[224:227], a[228:231], v[192:207]\n"
    "ds_read_b128 v[248:251], v211 offset:0\n"
    "s_waitcnt lgkmcnt(6)\n"
    "v_add_f32 v132, v132, v52\n"
    "v_add_f32 v133, v133, v53\n"
    "v_add_f32 v134, v134, v54\n"
    "v_add_f32 v135, v135, v55\n"
    "s_waitcnt lgkmcnt(3)\n"
    "v_mfma_f32_32x32x16_bf16 v[192:207], a[232:235], a[236:239], v[192:207]\n"
    "ds_read_b128 v[252:255], v211 offset:16\n"
    "s_waitcnt lgkmcnt(3)\n"
    "v_add_f32 v136, v136, v56\n"
    "v_add_f32 v137, v137, v57\n"
    "v_add_f32 v138, v138, v58\n"
    "v_add_f32 v139, v139, v59\n"
    "s_waitcnt lgkmcnt(2)\n"
    "v_add_f32 v140, v140, v60\n"
    "v_add_f32 v141, v141, v61\n"
    "v_add_f32 v142, v142, v62\n"
    "v_add_f32 v143, v143, v63\n"
    "v_mul_f32 v64, s0, v128\n"
    "v_mul_f32 v65, s0, v129\n"
    "v_mul_f32 v66, s0, v130\n"
    "v_mul_f32 v67, s0, v131\n"
    "v_mul_f32 v68, s0, v132\n"
    "v_mul_f32 v69, s0, v133\n"
    "v_mul_f32 v70, s0, v134\n"
    "v_mul_f32 v71, s0, v135\n"
    "v_mul_f32 v72, s0, v136\n"
    "v_exp_f32 v64, v64\n"
    "v_mul_f32 v73, s0, v137\n"
    "v_exp_f32 v65, v65\n"
    "v_mul_f32 v74, s0, v138\n"
    "v_exp_f32 v66, v66\n"
    "v_mul_f32 v75, s0, v139\n"
    "v_exp_f32 v67, v67\n"
    "v_mul_f32 v76, s0, v140\n"
    "v_exp_f32 v68, v68\n"
    "v_mul_f32 v77, s0, v141\n"
    "v_exp_f32 v69, v69\n"
    "v_mul_f32 v78, s0, v142\n"
    "v_exp_f32 v70, v70\n"
    "v_mul_f32 v79, s0, v143\n"
    "v_exp_f32 v71, v71\n"
    "ds_write_b128 v14, v[192:195] offset:0\n"
    "v_exp_f32 v72, v72\n"
    "v_exp_f32 v73, v73\n"
    "ds_write_b128 v14, v[196:199] offset:1024\n"
    "v_exp_f32 v74, v74\n"
    "v_exp_f32 v75, v75\n"
    "ds_write_b128 v14, v[200:203] offset:2048\n"
    "v_exp_f32 v76, v76\n"
    "v_exp_f32 v77, v77\n"
    "ds_write_b128 v14, v[204:207] offset:3072\n"
    "v_exp_f32 v78, v78\n"
    "v_exp_f32 v79, v79\n"
    "v_pk_add_f32 v[64:65], v[64:65], 1.0 op_sel_hi:[1,0]\n"
    "v_pk_add_f32 v[66:67], v[66:67], 1.0 op_sel_hi:[1,0]\n"
    "v_pk_add_f32 v[68:69], v[68:69], 1.0 op_sel_hi:[1,0]\n"
    "v_pk_add_f32 v[70:71], v[70:71], 1.0 op_sel_hi:[1,0]\n"
    "v_pk_add_f32 v[72:73], v[72:73], 1.0 op_sel_hi:[1,0]\n"
    "v_pk_add_f32 v[74:75], v[74:75], 1.0 op_sel_hi:[1,0]\n"
    "v_pk_add_f32 v[76:77], v[76:77], 1.0 op_sel_hi:[1,0]\n"
    "v_pk_add_f32 v[78:79], v[78:79], 1.0 op_sel_hi:[1,0]\n"
    "s_waitcnt lgkmcnt(0)\n"
    "s_barrier\n"
    "ds_read_b128 v[96:99], v15 offset:0\n"
    "ds_read_b128 v[100:103], v15 offset:1024\n"
    "ds_read_b128 v[104:107], v15 offset:2048\n"
    "ds_read_b128 v[108:111], v15 offset:3072\n"
    "v_rcp_f32 v64, v64\n"
    "v_rcp_f32 v65, v65\n"
    "v_rcp_f32 v66, v66\n"
    "v_rcp_f32 v67, v67\n"
    "v_rcp_f32 v68, v68\n"
    "v_rcp_f32 v69, v69\n"
    "v_rcp_f32 v70, v70\n"
    "v_rcp_f32 v71, v71\n"
    "v_lshlrev_b32 v80, 16, v248\n"
    "v_and_b32 v81, s77, v248\n"
    "v_lshlrev_b32 v82, 16, v249\n"
    "v_and_b32 v83, s77, v249\n"
    "v_lshlrev_b32 v84, 16, v250\n"
    "v_and_b32 v85, s77, v250\n"
    "v_lshlrev_b32 v86, 16, v251\n"
    "v_and_b32 v87, s77, v251\n"
    "s_waitcnt lgkmcnt(3)\n"
    "v_pk_fma_f32 v[96:97], v[192:193], s[76:77], v[96:97] op_sel_hi:[1,0,1]\n"
    "v_pk_fma_f32 v[98:99], v[194:195], s[76:77], v[98:99] op_sel_hi:[1,0,1]\n"
    "s_waitcnt lgkmcnt(2)\n"
    "v_pk_fma_f32 v[100:101], v[196:197], s[76:77], v[100:101] op_sel_hi:[1,0,1]\n"
    "v_pk_fma_f32 v[102:103], v[198:199], s[76:77], v[102:103] op_sel_hi:[1,0,1]\n"
    "v_pk_mul_f32 v[128:129], v[128:129], v[64:65]\n"
    "v_pk_mul_f32 v[130:131], v[130:131], v[66:67]\n"
    "v_pk_mul_f32 v[132:133], v[132:133], v[68:69]\n"
    "v_pk_mul_f32 v[134:135], v[134:135], v[70:71]\n"
    "v_pk_fma_f32 v[96:97], v[96:97], v[128:129], v[80:81]\n"
    "v_pk_fma_f32 v[98:99], v[98:99], v[130:131], v[82:83]\n"
    "v_pk_fma_f32 v[100:101], v[100:101], v[132:133], v[84:85]\n"
    "v_pk_fma_f32 v[102:103], v[102:103], v[134:135], v[86:87]\n"
    "v_cvt_pk_bf16_f32 v16, v96, v97\n"
    "v_cvt_pk_bf16_f32 v17, v98, v99\n"
    "v_cvt_pk_bf16_f32 v18, v100, v101\n"
    "v_cvt_pk_bf16_f32 v19, v102, v103\n"
    "buffer_store_dwordx4 v[16:19], v10, s[40:43], s46 offen offset:0\n"
    "v_rcp_f32 v72, v72\n"
    "v_rcp_f32 v73, v73\n"
    "v_rcp_f32 v74, v74\n"
    "v_rcp_f32 v75, v75\n"
    "v_rcp_f32 v76, v76\n"
    "v_rcp_f32 v77, v77\n"
    "v_rcp_f32 v78, v78\n"
    "v_rcp_f32 v79, v79\n"
    "v_lshlrev_b32 v88, 16, v252\n"
    "v_and_b32 v89, s77, v252\n"
    "v_lshlrev_b32 v90, 16, v253\n"
    "v_and_b32 v91, s77, v253\n"
    "v_lshlrev_b32 v92, 16, v254\n"
    "v_and_b32 v93, s77, v254\n"
    "v_lshlrev_b32 v94, 16, v255\n"
    "v_and_b32 v95, s77, v255\n"
    "s_waitcnt lgkmcnt(1)\n"
    "v_pk_fma_f32 v[104:105], v[200:201], s[76:77], v[104:105] op_sel_hi:[1,0,1]\n"
    "v_pk_fma_f32 v[106:107], v[202:203], s[76:77], v[106:107] op_sel_hi:[1,0,1]\n"
    "s_waitcnt lgkmcnt(0)\n"
    "v_pk_fma_f32 v[108:109], v[204:205], s[76:77], v[108:109] op_sel_hi:[1,0,1]\n"
    "v_pk_fma_f32 v[110:111], v[206:207], s[76:77], v[110:111] op_sel_hi:[1,0,1]\n"
    "v_pk_mul_f32 v[136:137], v[136:137], v[72:73]\n"
    "v_pk_mul_f32 v[138:139], v[138:139], v[74:75]\n"
    "v_pk_mul_f32 v[140:141], v[140:141], v[76:77]\n"
    "v_pk_mul_f32 v[142:143], v[142:143], v[78:79]\n"
    "v_pk_fma_f32 v[104:105], v[104:105], v[136:137], v[88:89]\n"
    "v_pk_fma_f32 v[106:107], v[106:107], v[138:139], v[90:91]\n"
    "v_pk_fma_f32 v[108:109], v[108:109], v[140:141], v[92:93]\n"
    "v_pk_fma_f32 v[110:111], v[110:111], v[142:143], v[94:95]\n"
    "v_cvt_pk_bf16_f32 v20, v104, v105\n"
    "v_cvt_pk_bf16_f32 v21, v106, v107\n"
    "v_cvt_pk_bf16_f32 v22, v108, v109\n"
    "v_cvt_pk_bf16_f32 v23, v110, v111\n"
    "buffer_store_dwordx4 v[20:23], v10, s[40:43], s46 offen offset:16\n"
    "s_endpgm\n"
    "SPIN4_%=:\n"
    "s_mov_b32 s70, 0\n"
    "LOOP5_%=:\n"
    "s_sleep 4\n"
    "buffer_load_dwordx2 v[240:241], v216, s[32:35], 0 offen sc1\n"
    "s_waitcnt vmcnt(0)\n"
    "v_cmp_ne_u64 vcc, s[60:61], v[240:241]\n"
    "s_nop 1\n"
    "s_cmp_lg_u64 vcc, 0\n"
    "s_cbranch_scc0 BACK6_%=\n"
    "s_add_u32 s70, s70, 1\n"
    "s_cmp_lt_u32 s70, 20000\n"
    "s_cbranch_scc1 LOOP5_%=\n"
    "s_endpgm\n"
    "SPIN8_%=:\n"
    "s_mov_b32 s70, 0\n"
    "LOOP9_%=:\n"
    "s_sleep 4\n"
    "buffer_load_dwordx2 v[240:241], v217, s[32:35], 0 offen sc1\n"
    "s_waitcnt vmcnt(0)\n"
    "v_cmp_ne_u64 vcc, s[60:61], v[240:241]\n"
    "s_nop 1\n"
    "s_cmp_lg_u64 vcc, 0\n"
    "s_cbranch_scc0 BACK10_%=\n"
    "s_add_u32 s70, s70, 1\n"
    "s_cmp_lt_u32 s70, 20000\n"
    "s_cbranch_scc1 LOOP9_%=\n"
    "s_endpgm\n"
    "SPIN13_%=:\n"
    "s_mov_b32 s70, 0\n"
    "LOOP14_%=:\n"
    "s_sleep 4\n"
    "buffer_load_dwordx2 v[240:241], v221, s[32:35], 0 offen sc1\n"
    "s_waitcnt vmcnt(0)\n"
    "v_cmp_ne_u64 vcc, s[60:61], v[240:241]\n"
    "s_nop 1\n"
    "s_cmp_lg_u64 vcc, 0\n"
    "s_cbranch_scc0 BACK15_%=\n"
    "s_add_u32 s70, s70, 1\n"
    "s_cmp_lt_u32 s70, 20000\n"
    "s_cbranch_scc1 LOOP14_%=\n"
    "s_endpgm\n"
    "SPIN18_%=:\n"
    "s_mov_b32 s70, 0\n"
    "LOOP19_%=:\n"
    "s_sleep 4\n"
    "buffer_load_dwordx2 v[240:241], v221, s[32:35], 0 offen sc1\n"
    "s_waitcnt vmcnt(0)\n"
    "v_cmp_ne_u64 vcc, s[60:61], v[240:241]\n"
    "s_nop 1\n"
    "s_cmp_lg_u64 vcc, 0\n"
    "s_cbranch_scc0 BACK20_%=\n"
    "s_add_u32 s70, s70, 1\n"
    "s_cmp_lt_u32 s70, 20000\n"
    "s_cbranch_scc1 LOOP19_%=\n"
    "s_endpgm\n"
    "SPIN22_%=:\n"
    "s_mov_b32 s70, 0\n"
    "LOOP23_%=:\n"
    "s_sleep 4\n"
    "buffer_load_dwordx4 v[240:243], v208, s[32:35], 0 offen sc1\n"
    "buffer_load_dwordx4 v[244:247], v209, s[32:35], 0 offen sc1\n"
    "s_waitcnt vmcnt(0)\n"
    "v_cmp_ne_u64 vcc, s[60:61], v[240:241]\n"
    "v_cmp_ne_u64 s[72:73], s[60:61], v[242:243]\n"
    "s_nop 1\n"
    "s_or_b64 vcc, vcc, s[72:73]\n"
    "v_cmp_ne_u64 s[72:73], s[60:61], v[244:245]\n"
    "s_nop 1\n"
    "s_or_b64 vcc, vcc, s[72:73]\n"
    "v_cmp_ne_u64 s[72:73], s[60:61], v[246:247]\n"
    "s_nop 1\n"
    "s_or_b64 vcc, vcc, s[72:73]\n"
    "s_nop 1\n"
    "s_cmp_lg_u64 vcc, 0\n"
    "s_cbranch_scc0 BACK24_%=\n"
    "s_add_u32 s70, s70, 1\n"
    "s_cmp_lt_u32 s70, 20000\n"
    "s_cbranch_scc1 LOOP23_%=\n"
    "s_endpgm\n"
    "AUX2_%=:\n"
    "; ==== aux ====\n"
    "s_sub_u32 s53, s2, 224\n"
    "s_waitcnt lgkmcnt(0)\n"
    "s_mov_b32 s24, s14\n"
    "s_and_b32 s25, s15, 0xffff\n"
    "s_mov_b32 s26, 1605632\n"
    "s_mov_b32 s27, 0x20000\n"
    "s_load_dwordx2 s[62:63], s[18:19], 0x0\n"
    "s_mul_i32 s45, s44, 14336\n"
    "s_mul_i32 s52, s53, 57344\n"
    "s_add_u32 s52, s52, s45\n"
    "s_add_u32 m0, s45, 0\n"
    "s_nop 0\n"
    "buffer_load_dwordx4 v1, s[24:27], s52 offen lds\n"
    "s_add_u32 m0, s45, 1024\n"
    "s_add_u32 s51, s52, 1024\n"
    "buffer_load_dwordx4 v1, s[24:27], s51 offen lds\n"
    "s_add_u32 m0, s45, 2048\n"
    "s_add_u32 s51, s52, 2048\n"
    "buffer_load_dwordx4 v1, s[24:27], s51 offen lds\n"
    "s_add_u32 m0, s45, 3072\n"
    "s_add_u32 s51, s52, 3072\n"
    "buffer_load_dwordx4 v1, s[24:27], s51 offen lds\n"
    "s_add_u32 m0, s45, 4096\n"
    "s_add_u32 s51, s52, 4096\n"
    "buffer_load_dwordx4 v1, s[24:27], s51 offen lds\n"
    "s_add_u32 m0, s45, 5120\n"
    "s_add_u32 s51, s52, 5120\n"
    "buffer_load_dwordx4 v1, s[24:27], s51 offen lds\n"
    "s_add_u32 m0, s45, 6144\n"
    "s_add_u32 s51, s52, 6144\n"
    "buffer_load_dwordx4 v1, s[24:27], s51 offen lds\n"
    "s_add_u32 m0, s45, 7168\n"
    "s_add_u32 s51, s52, 7168\n"
    "buffer_load_dwordx4 v1, s[24:27], s51 offen lds\n"
    "s_add_u32 m0, s45, 8192\n"
    "s_add_u32 s51, s52, 8192\n"
    "buffer_load_dwordx4 v1, s[24:27], s51 offen lds\n"
    "s_add_u32 m0, s45, 9216\n"
    "s_add_u32 s51, s52, 9216\n"
    "buffer_load_dwordx4 v1, s[24:27], s51 offen lds\n"
    "s_add_u32 m0, s45, 10240\n"
    "s_add_u32 s51, s52, 10240\n"
    "buffer_load_dwordx4 v1, s[24:27], s51 offen lds\n"
    "s_add_u32 m0, s45, 11264\n"
    "s_add_u32 s51, s52, 11264\n"
    "buffer_load_dwordx4 v1, s[24:27], s51 offen lds\n"
    "s_add_u32 m0, s45, 12288\n"
    "s_add_u32 s51, s52, 12288\n"
    "buffer_load_dwordx4 v1, s[24:27], s51 offen lds\n"
    "s_add_u32 m0, s45, 13312\n"
    "s_add_u32 s51, s52, 13312\n"
    "buffer_load_dwordx4 v1, s[24:27], s51 offen lds\n"
    "s_cmp_lt_u32 s44, 2\n"
    "s_cselect_b64 s[72:73], s[6:7], s[8:9]\n"
    "s_mov_b32 s28, s72\n"
    "s_and_b32 s29, s73, 0xffff\n"
    "s_mov_b32 s30, 921600\n"
    "s_mov_b32 s31, 0x20000\n"
    "s_mov_b32 s32, s18\n"
    "s_and_b32 s33, s19, 0xffff\n"
    "s_mov_b32 s34, 15433728\n"
    "s_mov_b32 s35, 0x20000\n"
    "s_mov_b32 s36, s20\n"
    "s_and_b32 s37, s21, 0xffff\n"
    "s_mov_b32 s38, 624640\n"
    "s_mov_b32 s39, 0x20000\n"
    "s_lshr_b32 s54, s53, 2\n"
    "s_and_b32 s55, s53, 3\n"
    "s_and_b32 s58, s44, 1\n"
    "s_lshl1_add_u32 s52, s54, s58\n"
    "s_lshl_b32 s52, s52, 3\n"
    "s_add_u32 s52, s52, s55\n"
    "s_mul_i32 s52, s52, 7200\n"
    "s_lshl_b32 s71, s44, 14\n"
    "s_mov_b32 s66, s52\n"
    "s_add_u32 m0, s71, 57344\n"
    "s_nop 0\n"
    "buffer_load_dwordx4 v1, s[28:31], s66 offen lds\n"
    "s_add_u32 m0, s71, 58368\n"
    "s_add_u32 s51, s66, 1024\n"
    "buffer_load_dwordx4 v1, s[28:31], s51 offen lds\n"
    "s_add_u32 m0, s71, 59392\n"
    "s_add_u32 s51, s66, 2048\n"
    "buffer_load_dwordx4 v1, s[28:31], s51 offen lds\n"
    "s_add_u32 m0, s71, 60416\n"
    "s_add_u32 s51, s66, 3072\n"
    "buffer_load_dwordx4 v1, s[28:31], s51 offen lds\n"
    "s_add_u32 m0, s71, 61440\n"
    "s_add_u32 s51, s66, 4096\n"
    "buffer_load_dwordx4 v1, s[28:31], s51 offen lds\n"
    "s_add_u32 m0, s71, 62464\n"
    "s_add_u32 s51, s66, 5120\n"
    "buffer_load_dwordx4 v1, s[28:31], s51 offen lds\n"
    "s_add_u32 m0, s71, 63488\n"
    "s_add_u32 s51, s66, 6144\n"
    "buffer_load_dwordx4 v1, s[28:31], s51 offen lds\n"
    "s_add_u32 m0, s71, 64512\n"
    "s_add_u32 s51, s66, 7168\n"
    "buffer_load_dwordx4 v1, s[28:31], s51 offen lds\n"
    "s_add_u32 m0, s71, 65536\n"
    "s_add_u32 s51, s66, 28800\n"
    "buffer_load_dwordx4 v1, s[28:31], s51 offen lds\n"
    "s_add_u32 m0, s71, 66560\n"
    "s_add_u32 s51, s66, 29824\n"
    "buffer_load_dwordx4 v1, s[28:31], s51 offen lds\n"
    "s_add_u32 m0, s71, 67584\n"
    "s_add_u32 s51, s66, 30848\n"
    "buffer_load_dwordx4 v1, s[28:31], s51 offen lds\n"
    "s_add_u32 m0, s71, 68608\n"
    "s_add_u32 s51, s66, 31872\n"
    "buffer_load_dwordx4 v1, s[28:31], s51 offen lds\n"
    "s_add_u32 m0, s71, 69632\n"
    "s_add_u32 s51, s66, 32896\n"
    "buffer_load_dwordx4 v1, s[28:31], s51 offen lds\n"
    "s_add_u32 m0, s71, 70656\n"
    "s_add_u32 s51, s66, 33920\n"
    "buffer_load_dwordx4 v1, s[28:31], s51 offen lds\n"
    "s_add_u32 m0, s71, 71680\n"
    "s_add_u32 s51, s66, 34944\n"
    "buffer_load_dwordx4 v1, s[28:31], s51 offen lds\n"
    "s_add_u32 m0, s71, 72704\n"
    "s_add_u32 s51, s66, 35968\n"
    "buffer_load_dwordx4 v1, s[28:31], s51 offen lds\n"
    "s_mov_b32 s77, 0xFFFF0000\n"
    "s_waitcnt lgkmcnt(0)\n"
    "s_mul_i32 s60, s62, 0x9E3779B1\n"
    "s_add_u32 s60, s60, 0x7F4A7C15\n"
    "s_mul_i32 s61, s63, 0x85EBCA77\n"
    "s_add_u32 s61, s61, 0x165667B1\n"
    "v_mov_b32 v222, s60\n"
    "v_mov_b32 v223, s61\n"
    "v_lshrrev_b32 v208, 5, v0\n"
    "v_bfe_u32 v209, v0, 2, 2\n"
    "v_lshl_add_u32 v208, v208, 2, v209\n"
    "v_mul_u32_u24 v2, 896, v208\n"
    "v_add_u32 v2, s45, v2\n"
    "v_and_b32 v208, 1, v0\n"
    "v_lshl_add_u32 v2, v208, 5, v2\n"
    "v_bfe_u32 v208, v0, 4, 1\n"
    "v_lshl_add_u32 v2, v208, 4, v2\n"
    "v_bfe_u32 v208, v0, 1, 1\n"
    "v_lshl_add_u32 v2, v208, 3, v2\n"
    "s_waitcnt vmcnt(16)\n"
    "s_cmp_ge_u32 s53, 28\n"
    "s_cbranch_scc1 NOWD1003_%=\n"
    "s_and_b32 s59, s53, 1\n"
    "s_mul_i32 s59, s59, 14\n"
    "s_lshr_b32 s52, s53, 1\n"
    "s_add_u32 s59, s59, s52\n"
    "s_mul_i32 s52, s44, 28\n"
    "s_add_u32 s59, s59, s52\n"
    "s_lshl_b32 s59, s59, 11\n"
    "ds_read_b64_tr_b16 v[16:17], v2 offset:0\n"
    "ds_read_b64_tr_b16 v[18:19], v2 offset:7168\n"
    "ds_read_b64_tr_b16 v[20:21], v2 offset:448\n"
    "ds_read_b64_tr_b16 v[22:23], v2 offset:7616\n"
    "ds_read_b64_tr_b16 v[24:25], v2 offset:64\n"
    "ds_read_b64_tr_b16 v[26:27], v2 offset:7232\n"
    "ds_read_b64_tr_b16 v[28:29], v2 offset:512\n"
    "ds_read_b64_tr_b16 v[30:31], v2 offset:7680\n"
    "s_add_u32 s51, s59, 11272192\n"
    "s_waitcnt lgkmcnt(6)\n"
    "buffer_store_dwordx4 v[16:19], v1, s[32:35], s51 offen sc1\n"
    "ds_read_b64_tr_b16 v[32:33], v2 offset:128\n"
    "ds_read_b64_tr_b16 v[34:35], v2 offset:7296\n"
    "s_add_u32 s51, s59, 11273216\n"
    "s_waitcnt lgkmcnt(6)\n"
    "buffer_store_dwordx4 v[20:23], v1, s[32:35], s51 offen sc1\n"
    "ds_read_b64_tr_b16 v[36:37], v2 offset:576\n"
    "ds_read_b64_tr_b16 v[38:39], v2 offset:7744\n"
    "s_add_u32 s51, s59, 11501568\n"
    "s_waitcnt lgkmcnt(6)\n"
    "buffer_store_dwordx4 v[24:27], v1, s[32:35], s51 offen sc1\n"
    "ds_read_b64_tr_b16 v[40:41], v2 offset:192\n"
    "ds_read_b64_tr_b16 v[42:43], v2 offset:7360\n"
    "s_add_u32 s51, s59, 11502592\n"
    "s_waitcnt lgkmcnt(6)\n"
    "buffer_store_dwordx4 v[28:31], v1, s[32:35], s51 offen sc1\n"
    "ds_read_b64_tr_b16 v[44:45], v2 offset:640\n"
    "ds_read_b64_tr_b16 v[46:47], v2 offset:7808\n"
    "s_add_u32 s51, s59, 11730944\n"
    "s_waitcnt lgkmcnt(6)\n"
    "buffer_store_dwordx4 v[32:35], v1, s[32:35], s51 offen sc1\n"
    "ds_read_b64_tr_b16 v[48:49], v2 offset:256\n"
    "ds_read_b64_tr_b16 v[50:51], v2 offset:7424\n"
    "s_add_u32 s51, s59, 11731968\n"
    "s_waitcnt lgkmcnt(6)\n"
    "buffer_store_dwordx4 v[36:39], v1, s[32:35], s51 offen sc1\n"
    "ds_read_b64_tr_b16 v[52:53], v2 offset:704\n"
    "ds_read_b64_tr_b16 v[54:55], v2 offset:7872\n"
    "s_add_u32 s51, s59, 11960320\n"
    "s_waitcnt lgkmcnt(6)\n"
    "buffer_store_dwordx4 v[40:43], v1, s[32:35], s51 offen sc1\n"
    "ds_read_b64_tr_b16 v[56:57], v2 offset:320\n"
    "ds_read_b64_tr_b16 v[58:59], v2 offset:7488\n"
    "s_add_u32 s51, s59, 11961344\n"
    "s_waitcnt lgkmcnt(6)\n"
    "buffer_store_dwordx4 v[44:47], v1, s[32:35], s51 offen sc1\n"
    "ds_read_b64_tr_b16 v[60:61], v2 offset:768\n"
    "ds_read_b64_tr_b16 v[62:63], v2 offset:7936\n"
    "s_add_u32 s51, s59, 12189696\n"
    "s_waitcnt lgkmcnt(6)\n"
    "buffer_store_dwordx4 v[48:51], v1, s[32:35], s51 offen sc1\n"
    "ds_read_b64_tr_b16 v[64:65], v2 offset:384\n"
    "ds_read_b64_tr_b16 v[66:67], v2 offset:7552\n"
    "s_add_u32 s51, s59, 12190720\n"
    "s_waitcnt lgkmcnt(6)\n"
    "buffer_store_dwordx4 v[52:55], v1, s[32:35], s51 offen sc1\n"
    "ds_read_b64_tr_b16 v[68:69], v2 offset:832\n"
    "ds_read_b64_tr_b16 v[70:71], v2 offset:8000\n"
    "s_add_u32 s51, s59, 12419072\n"
    "s_waitcnt lgkmcnt(6)\n"
    "buffer_store_dwordx4 v[56:59], v1, s[32:35], s51 offen sc1\n"
    "s_add_u32 s51, s59, 12420096\n"
    "s_waitcnt lgkmcnt(4)\n"
    "buffer_store_dwordx4 v[60:63], v1, s[32:35], s51 offen sc1\n"
    "s_add_u32 s51, s59, 12648448\n"
    "s_waitcnt lgkmcnt(2)\n"
    "buffer_store_dwordx4 v[64:67], v1, s[32:35], s51 offen sc1\n"
    "s_add_u32 s51, s59, 12649472\n"
    "s_waitcnt lgkmcnt(0)\n"
    "buffer_store_dwordx4 v[68:71], v1, s[32:35], s51 offen sc1\n"
    "s_lshl_b32 s78, s53, 7\n"
    "s_add_u32 s78, s78, 4096\n"
    "s_waitcnt vmcnt(0) lgkmcnt(0)\n"
    "s_barrier\n"
    "s_cmp_lg_u32 s44, 0\n"
    "s_cbranch_scc1 NP1104_%=\n"
    "s_mov_b64 exec, 1\n"
    "buffer_store_dwordx2 v[222:223], off, s[32:35], s78 sc1\n"
    "s_mov_b64 exec, -1\n"
    "NP1104_%=:\n"
    "NOWD1003_%=:\n"
    "s_lshl2_add_u32 s52, s53, s44\n"
    "s_cmp_lt_u32 s52, 64\n"
    "s_cselect_b64 s[72:73], s[10:11], s[12:13]\n"
    "s_mov_b32 s40, s72\n"
    "s_and_b32 s41, s73, 0xffff\n"
    "s_mov_b32 s42, 1605632\n"
    "s_mov_b32 s43, 0x20000\n"
    "s_and_b32 s52, s52, 63\n"
    "s_mul_i32 s52, s52, 98\n"
    "v_add_u32 v128, 0, v0\n"
    "v_min_u32 v128, 97, v128\n"
    "v_add_u32 v128, s52, v128\n"
    "v_lshlrev_b32 v128, 8, v128\n"
    "buffer_load_dword v129, v128, s[40:43], 0 offen offset:128\n"
    "v_add_u32 v130, 64, v0\n"
    "v_min_u32 v130, 97, v130\n"
    "v_add_u32 v130, s52, v130\n"
    "v_lshlrev_b32 v130, 8, v130\n"
    "buffer_load_dword v131, v130, s[40:43], 0 offen offset:128\n"
    "s_waitcnt vmcnt(2)\n"
    "s_barrier\n"
    "v_and_b32 v208, 15, v0\n"
    "v_lshrrev_b32 v209, 4, v0\n"
    "v_lshl_add_u32 v209, s44, 2, v209\n"
    "v_mul_u32_u24 v3, 450, v208\n"
    "v_lshl_add_u32 v3, v209, 1, v3\n"
    "v_add_u32 v3, 57344, v3\n"
    "v_lshlrev_b32 v4, 13, v209\n"
    "v_lshl_add_u32 v4, v208, 4, v4\n"
    "s_lshl_b32 s59, s55, 11\n"
    "s_lshl_b32 s52, s54, 8\n"
    "s_add_u32 s59, s59, s52\n"
    "ds_read_u16 v16, v3 offset:0\n"
    "ds_read_u16 v17, v3 offset:32768\n"
    "ds_read_u16 v18, v3 offset:8192\n"
    "ds_read_u16 v19, v3 offset:40960\n"
    "ds_read_u16 v20, v3 offset:16384\n"
    "ds_read_u16 v21, v3 offset:49152\n"
    "ds_read_u16 v22, v3 offset:24576\n"
    "ds_read_u16 v23, v3 offset:57344\n"
    "s_waitcnt lgkmcnt(0)\n"
    "ds_read_u16 v24, v3 offset:32\n"
    "ds_read_u16 v25, v3 offset:32800\n"
    "ds_read_u16 v26, v3 offset:8224\n"
    "ds_read_u16 v27, v3 offset:40992\n"
    "ds_read_u16 v28, v3 offset:16416\n"
    "ds_read_u16 v29, v3 offset:49184\n"
    "ds_read_u16 v30, v3 offset:24608\n"
    "ds_read_u16 v31, v3 offset:57376\n"
    "v_lshl_or_b32 v32, v17, 16, v16\n"
    "v_lshl_or_b32 v33, v19, 16, v18\n"
    "v_lshl_or_b32 v34, v21, 16, v20\n"
    "v_lshl_or_b32 v35, v23, 16, v22\n"
    "s_add_u32 s51, s59, 12877824\n"
    "buffer_store_dwordx4 v[32:35], v4, s[32:35], s51 offen sc1\n"
    "s_waitcnt lgkmcnt(0)\n"
    "ds_read_u16 v16, v3 offset:64\n"
    "ds_read_u16 v17, v3 offset:32832\n"
    "ds_read_u16 v18, v3 offset:8256\n"
    "ds_read_u16 v19, v3 offset:41024\n"
    "ds_read_u16 v20, v3 offset:16448\n"
    "ds_read_u16 v21, v3 offset:49216\n"
    "ds_read_u16 v22, v3 offset:24640\n"
    "ds_read_u16 v23, v3 offset:57408\n"
    "v_lshl_or_b32 v36, v25, 16, v24\n"
    "v_lshl_or_b32 v37, v27, 16, v26\n"
    "v_lshl_or_b32 v38, v29, 16, v28\n"
    "v_lshl_or_b32 v39, v31, 16, v30\n"
    "s_add_u32 s51, s59, 13008896\n"
    "buffer_store_dwordx4 v[36:39], v4, s[32:35], s51 offen sc1\n"
    "s_waitcnt lgkmcnt(0)\n"
    "ds_read_u16 v24, v3 offset:96\n"
    "ds_read_u16 v25, v3 offset:32864\n"
    "ds_read_u16 v26, v3 offset:8288\n"
    "ds_read_u16 v27, v3 offset:41056\n"
    "ds_read_u16 v28, v3 offset:16480\n"
    "ds_read_u16 v29, v3 offset:49248\n"
    "ds_read_u16 v30, v3 offset:24672\n"
    "ds_read_u16 v31, v3 offset:57440\n"
    "v_lshl_or_b32 v40, v17, 16, v16\n"
    "v_lshl_or_b32 v41, v19, 16, v18\n"
    "v_lshl_or_b32 v42, v21, 16, v20\n"
    "v_lshl_or_b32 v43, v23, 16, v22\n"
    "s_add_u32 s51, s59, 13139968\n"
    "buffer_store_dwordx4 v[40:43], v4, s[32:35], s51 offen sc1\n"
    "s_waitcnt lgkmcnt(0)\n"
    "ds_read_u16 v16, v3 offset:128\n"
    "ds_read_u16 v17, v3 offset:32896\n"
    "ds_read_u16 v18, v3 offset:8320\n"
    "ds_read_u16 v19, v3 offset:41088\n"
    "ds_read_u16 v20, v3 offset:16512\n"
    "ds_read_u16 v21, v3 offset:49280\n"
    "ds_read_u16 v22, v3 offset:24704\n"
    "ds_read_u16 v23, v3 offset:57472\n"
    "v_lshl_or_b32 v44, v25, 16, v24\n"
    "v_lshl_or_b32 v45, v27, 16, v26\n"
    "v_lshl_or_b32 v46, v29, 16, v28\n"
    "v_lshl_or_b32 v47, v31, 16, v30\n"
    "s_add_u32 s51, s59, 13271040\n"
    "buffer_store_dwordx4 v[44:47], v4, s[32:35], s51 offen sc1\n"
    "s_waitcnt lgkmcnt(0)\n"
    "ds_read_u16 v24, v3 offset:160\n"
    "ds_read_u16 v25, v3 offset:32928\n"
    "ds_read_u16 v26, v3 offset:8352\n"
    "ds_read_u16 v27, v3 offset:41120\n"
    "ds_read_u16 v28, v3 offset:16544\n"
    "ds_read_u16 v29, v3 offset:49312\n"
    "ds_read_u16 v30, v3 offset:24736\n"
    "ds_read_u16 v31, v3 offset:57504\n"
    "v_lshl_or_b32 v32, v17, 16, v16\n"
    "v_lshl_or_b32 v33, v19, 16, v18\n"
    "v_lshl_or_b32 v34, v21, 16, v20\n"
    "v_lshl_or_b32 v35, v23, 16, v22\n"
    "s_add_u32 s51, s59, 13402112\n"
    "buffer_store_dwordx4 v[32:35], v4, s[32:35], s51 offen sc1\n"
    "s_waitcnt lgkmcnt(0)\n"
    "ds_read_u16 v16, v3 offset:192\n"
    "ds_read_u16 v17, v3 offset:32960\n"
    "ds_read_u16 v18, v3 offset:8384\n"
    "ds_read_u16 v19, v3 offset:41152\n"
    "ds_read_u16 v20, v3 offset:16576\n"
    "ds_read_u16 v21, v3 offset:49344\n"
    "ds_read_u16 v22, v3 offset:24768\n"
    "ds_read_u16 v23, v3 offset:57536\n"
    "v_lshl_or_b32 v36, v25, 16, v24\n"
    "v_lshl_or_b32 v37, v27, 16, v26\n"
    "v_lshl_or_b32 v38, v29, 16, v28\n"
    "v_lshl_or_b32 v39, v31, 16, v30\n"
    "s_add_u32 s51, s59, 13533184\n"
    "buffer_store_dwordx4 v[36:39], v4, s[32:35], s51 offen sc1\n"
    "s_waitcnt lgkmcnt(0)\n"
    "ds_read_u16 v24, v3 offset:224\n"
    "ds_read_u16 v25, v3 offset:32992\n"
    "ds_read_u16 v26, v3 offset:8416\n"
    "ds_read_u16 v27, v3 offset:41184\n"
    "ds_read_u16 v28, v3 offset:16608\n"
    "ds_read_u16 v29, v3 offset:49376\n"
    "ds_read_u16 v30, v3 offset:24800\n"
    "ds_read_u16 v31, v3 offset:57568\n"
    "v_lshl_or_b32 v40, v17, 16, v16\n"
    "v_lshl_or_b32 v41, v19, 16, v18\n"
    "v_lshl_or_b32 v42, v21, 16, v20\n"
    "v_lshl_or_b32 v43, v23, 16, v22\n"
    "s_add_u32 s51, s59, 13664256\n"
    "buffer_store_dwordx4 v[40:43], v4, s[32:35], s51 offen sc1\n"
    "s_waitcnt lgkmcnt(0)\n"
    "ds_read_u16 v16, v3 offset:256\n"
    "ds_read_u16 v17, v3 offset:33024\n"
    "ds_read_u16 v18, v3 offset:8448\n"
    "ds_read_u16 v19, v3 offset:41216\n"
    "ds_read_u16 v20, v3 offset:16640\n"
    "ds_read_u16 v21, v3 offset:49408\n"
    "ds_read_u16 v22, v3 offset:24832\n"
    "ds_read_u16 v23, v3 offset:57600\n"
    "v_lshl_or_b32 v44, v25, 16, v24\n"
    "v_lshl_or_b32 v45, v27, 16, v26\n"
    "v_lshl_or_b32 v46, v29, 16, v28\n"
    "v_lshl_or_b32 v47, v31, 16, v30\n"
    "s_add_u32 s51, s59, 13795328\n"
    "buffer_store_dwordx4 v[44:47], v4, s[32:35], s51 offen sc1\n"
    "s_waitcnt lgkmcnt(0)\n"
    "ds_read_u16 v24, v3 offset:288\n"
    "ds_read_u16 v25, v3 offset:33056\n"
    "ds_read_u16 v26, v3 offset:8480\n"
    "ds_read_u16 v27, v3 offset:41248\n"
    "ds_read_u16 v28, v3 offset:16672\n"
    "ds_read_u16 v29, v3 offset:49440\n"
    "ds_read_u16 v30, v3 offset:24864\n"
    "ds_read_u16 v31, v3 offset:57632\n"
    "v_lshl_or_b32 v32, v17, 16, v16\n"
    "v_lshl_or_b32 v33, v19, 16, v18\n"
    "v_lshl_or_b32 v34, v21, 16, v20\n"
    "v_lshl_or_b32 v35, v23, 16, v22\n"
    "s_add_u32 s51, s59, 13926400\n"
    "buffer_store_dwordx4 v[32:35], v4, s[32:35], s51 offen sc1\n"
    "s_waitcnt lgkmcnt(0)\n"
    "ds_read_u16 v16, v3 offset:320\n"
    "ds_read_u16 v17, v3 offset:33088\n"
    "ds_read_u16 v18, v3 offset:8512\n"
    "ds_read_u16 v19, v3 offset:41280\n"
    "ds_read_u16 v20, v3 offset:16704\n"
    "ds_read_u16 v21, v3 offset:49472\n"
    "ds_read_u16 v22, v3 offset:24896\n"
    "ds_read_u16 v23, v3 offset:57664\n"
    "v_lshl_or_b32 v36, v25, 16, v24\n"
    "v_lshl_or_b32 v37, v27, 16, v26\n"
    "v_lshl_or_b32 v38, v29, 16, v28\n"
    "v_lshl_or_b32 v39, v31, 16, v30\n"
    "s_add_u32 s51, s59, 14057472\n"
    "buffer_store_dwordx4 v[36:39], v4, s[32:35], s51 offen sc1\n"
    "s_waitcnt lgkmcnt(0)\n"
    "ds_read_u16 v24, v3 offset:352\n"
    "ds_read_u16 v25, v3 offset:33120\n"
    "ds_read_u16 v26, v3 offset:8544\n"
    "ds_read_u16 v27, v3 offset:41312\n"
    "ds_read_u16 v28, v3 offset:16736\n"
    "ds_read_u16 v29, v3 offset:49504\n"
    "ds_read_u16 v30, v3 offset:24928\n"
    "ds_read_u16 v31, v3 offset:57696\n"
    "v_lshl_or_b32 v40, v17, 16, v16\n"
    "v_lshl_or_b32 v41, v19, 16, v18\n"
    "v_lshl_or_b32 v42, v21, 16, v20\n"
    "v_lshl_or_b32 v43, v23, 16, v22\n"
    "s_add_u32 s51, s59, 14188544\n"
    "buffer_store_dwordx4 v[40:43], v4, s[32:35], s51 offen sc1\n"
    "s_waitcnt lgkmcnt(0)\n"
    "ds_read_u16 v16, v3 offset:384\n"
    "ds_read_u16 v17, v3 offset:33152\n"
    "ds_read_u16 v18, v3 offset:8576\n"
    "ds_read_u16 v19, v3 offset:41344\n"
    "ds_read_u16 v20, v3 offset:16768\n"
    "ds_read_u16 v21, v3 offset:49536\n"
    "ds_read_u16 v22, v3 offset:24960\n"
    "ds_read_u16 v23, v3 offset:57728\n"
    "v_lshl_or_b32 v44, v25, 16, v24\n"
    "v_lshl_or_b32 v45, v27, 16, v26\n"
    "v_lshl_or_b32 v46, v29, 16, v28\n"
    "v_lshl_or_b32 v47, v31, 16, v30\n"
    "s_add_u32 s51, s59, 14319616\n"
    "buffer_store_dwordx4 v[44:47], v4, s[32:35], s51 offen sc1\n"
    "s_waitcnt lgkmcnt(0)\n"
    "ds_read_u16 v24, v3 offset:416\n"
    "ds_read_u16 v25, v3 offset:33184\n"
    "ds_read_u16 v26, v3 offset:8608\n"
    "ds_read_u16 v27, v3 offset:41376\n"
    "ds_read_u16 v28, v3 offset:16800\n"
    "ds_read_u16 v29, v3 offset:49568\n"
    "ds_read_u16 v30, v3 offset:24992\n"
    "ds_read_u16 v31, v3 offset:57760\n"
    "v_lshl_or_b32 v32, v17, 16, v16\n"
    "v_lshl_or_b32 v33, v19, 16, v18\n"
    "v_lshl_or_b32 v34, v21, 16, v20\n"
    "v_lshl_or_b32 v35, v23, 16, v22\n"
    "s_add_u32 s51, s59, 14450688\n"
    "buffer_store_dwordx4 v[32:35], v4, s[32:35], s51 offen sc1\n"
    "s_waitcnt lgkmcnt(0)\n"
    "ds_read_u16 v16, v3 offset:448\n"
    "ds_read_u16 v17, v3 offset:33216\n"
    "ds_read_u16 v18, v3 offset:8640\n"
    "ds_read_u16 v19, v3 offset:41408\n"
    "ds_read_u16 v20, v3 offset:16832\n"
    "ds_read_u16 v21, v3 offset:49600\n"
    "ds_read_u16 v22, v3 offset:25024\n"
    "ds_read_u16 v23, v3 offset:57792\n"
    "v_lshl_or_b32 v36, v25, 16, v24\n"
    "v_lshl_or_b32 v37, v27, 16, v26\n"
    "v_lshl_or_b32 v38, v29, 16, v28\n"
    "v_lshl_or_b32 v39, v31, 16, v30\n"
    "s_add_u32 s51, s59, 14581760\n"
    "buffer_store_dwordx4 v[36:39], v4, s[32:35], s51 offen sc1\n"
    "s_waitcnt lgkmcnt(0)\n"
    "v_lshl_or_b32 v40, v17, 16, v16\n"
    "v_lshl_or_b32 v41, v19, 16, v18\n"
    "v_lshl_or_b32 v42, v21, 16, v20\n"
    "v_lshl_or_b32 v43, v23, 16, v22\n"
    "s_add_u32 s51, s59, 14712832\n"
    "buffer_store_dwordx4 v[40:43], v4, s[32:35], s51 offen sc1\n"
    "s_lshl_b32 s78, s53, 7\n"
    "s_add_u32 s78, s78, 8192\n"
    "s_waitcnt vmcnt(0) lgkmcnt(0)\n"
    "s_barrier\n"
    "s_cmp_lg_u32 s44, 0\n"
    "s_cbranch_scc1 NP1105_%=\n"
    "s_mov_b64 exec, 1\n"
    "buffer_store_dwordx2 v[222:223], off, s[32:35], s78 sc1\n"
    "s_mov_b64 exec, -1\n"
    "NP1105_%=:\n"
    "s_lshr_b32 s52, s53, 3\n"
    "s_lshl2_add_u32 s52, s52, s44\n"
    "s_lshl_b32 s52, s52, 14\n"
    "s_add_u32 s52, s52, 401408\n"
    "v_lshlrev_b32 v132, 7, v0\n"
    "buffer_load_dword v133, v132, s[36:39], s52 offen\n"
    "s_add_u32 s52, s52, 8192\n"
    "buffer_load_dword v134, v132, s[36:39], s52 offen\n"
    "s_cmp_lg_u32 s53, 31\n"
    "s_cbranch_scc1 AUXDONE1106_%=\n"
    "s_mov_b32 s69, 224\n"
    "s_lshl_b32 s74, s69, 1\n"
    "s_cmp_eq_u32 s69, 224\n"
    "s_cselect_b32 s74, 1, s74\n"
    "s_cmp_lt_u32 s74, 2\n"
    "s_cselect_b32 s75, 1879048192, 0\n"
    "v_add_u32 v13, 114688, v1\n"
    "s_mul_i32 s71, s44, 5120\n"
    "s_add_u32 m0, s71, 114688\n"
    "s_add_u32 s51, s71, 602112\n"
    "buffer_load_dwordx4 v1, s[36:39], s51 offen lds\n"
    "s_add_u32 m0, s71, 115712\n"
    "s_add_u32 s51, s71, 603136\n"
    "buffer_load_dwordx4 v1, s[36:39], s51 offen lds\n"
    "s_add_u32 m0, s71, 116736\n"
    "s_add_u32 s51, s71, 604160\n"
    "buffer_load_dwordx4 v1, s[36:39], s51 offen lds\n"
    "s_add_u32 m0, s71, 117760\n"
    "s_add_u32 s51, s71, 605184\n"
    "buffer_load_dwordx4 v1, s[36:39], s51 offen lds\n"
    "s_add_u32 m0, s71, 118784\n"
    "s_add_u32 s51, s71, 606208\n"
    "buffer_load_dwordx4 v1, s[36:39], s51 offen lds\n"
    "v_lshrrev_b32 v250, 3, v0\n"
    "v_and_b32 v251, 7, v0\n"
    "v_lshrrev_b32 v176, 4, v0\n"
    "v_xor_b32 v251, v251, v176\n"
    "v_lshlrev_b32 v251, 4, v251\n"
    "v_lshl_add_u32 v250, v250, 7, v251\n"
    "v_xor_b32 v251, 64, v250\n"
    "s_lshr_b32 s52, s44, 1\n"
    "s_add_u32 s98, s74, s52\n"
    "s_lshl_b32 s98, s98, 12\n"
    "s_mul_i32 s52, s52, s75\n"
    "s_add_u32 s98, s98, s52\n"
    "s_and_b32 s52, s44, 1\n"
    "s_lshl_b32 s52, s52, 11\n"
    "s_add_u32 s98, s98, s52\n"
    "s_add_u32 s98, s98, 262144\n"
    "s_lshl2_add_u32 s99, s69, s44\n"
    "s_lshl_b32 s99, s99, 11\n"
    "s_add_u32 s99, s99, 12877824\n"
    "s_mul_i32 s79, s44, 458752\n"
    "s_lshl_b32 s52, s74, 7\n"
    "s_add_u32 s79, s79, s52\n"
    "s_add_u32 s79, s79, 2097152\n"
    "v_and_b32 v176, 15, v0\n"
    "v_lshl_add_u32 v176, s44, 4, v176\n"
    "v_cvt_f32_u32 v176, v176\n"
    "v_mul_f32 v176, 0x3a000000, v176\n"
    "v_lshrrev_b32 v177, 4, v0\n"
    "v_lshlrev_b32 v177, 2, v177\n"
    "v_cvt_f32_u32 v177, v177\n"
    "v_mul_f32 v177, v176, v177\n"
    "v_fmamk_f32 v232, v176, 0x0, v177\n"
    "v_cos_f32 v224, v232\n"
    "v_sin_f32 v232, v232\n"
    "v_fmamk_f32 v233, v176, 0x3f800000, v177\n"
    "v_cos_f32 v225, v233\n"
    "v_sin_f32 v233, v233\n"
    "v_fmamk_f32 v234, v176, 0x40000000, v177\n"
    "v_cos_f32 v226, v234\n"
    "v_sin_f32 v234, v234\n"
    "v_fmamk_f32 v235, v176, 0x40400000, v177\n"
    "v_cos_f32 v227, v235\n"
    "v_sin_f32 v235, v235\n"
    "v_fmamk_f32 v236, v176, 0x41800000, v177\n"
    "v_cos_f32 v228, v236\n"
    "v_sin_f32 v236, v236\n"
    "v_fmamk_f32 v237, v176, 0x41880000, v177\n"
    "v_cos_f32 v229, v237\n"
    "v_sin_f32 v237, v237\n"
    "v_fmamk_f32 v238, v176, 0x41900000, v177\n"
    "v_cos_f32 v230, v238\n"
    "v_sin_f32 v238, v238\n"
    "v_fmamk_f32 v239, v176, 0x41980000, v177\n"
    "v_cos_f32 v231, v239\n"
    "v_sin_f32 v239, v239\n"
    "v_and_b32 v176, 15, v0\n"
    "v_lshrrev_b32 v177, 4, v0\n"
    "v_lshrrev_b32 v242, 1, v176\n"
    "v_xor_b32 v242, v242, v177\n"
    "v_lshlrev_b32 v242, 4, v242\n"
    "v_lshl_add_u32 v242, v176, 7, v242\n"
    "v_add_u32 v242, 135168, v242\n"
    "v_lshl_add_u32 v243, s44, 4, v176\n"
    "v_mul_u32_u24 v243, 72, v243\n"
    "v_lshl_add_u32 v243, v177, 3, v243\n"
    "v_add_u32 v243, 147456, v243\n"
    "v_lshrrev_b32 v247, 3, v0\n"
    "v_mul_u32_u24 v247, 57344, v247\n"
    "v_and_b32 v246, 7, v0\n"
    "v_lshl_add_u32 v247, v246, 4, v247\n"
    "v_mov_b32 v246, 0x80008000\n"
    "v_cmp_gt_u32 vcc, 8, v176\n"
    "s_nop 1\n"
    "v_cndmask_b32 v246, 0, v246, vcc\n"
    "v_and_b32 v244, 1, v177\n"
    "v_lshlrev_b32 v244, 4, v244\n"
    "v_lshrrev_b32 v177, 1, v177\n"
    "v_lshl_add_u32 v244, v177, 2, v244\n"
    "v_lshrrev_b32 v177, 2, v176\n"
    "v_add_u32 v244, v244, v177\n"
    "v_mul_u32_u24 v244, 72, v244\n"
    "v_and_b32 v177, 1, v0\n"
    "v_lshl_add_u32 v244, v177, 3, v244\n"
    "v_lshl_add_u32 v244, s44, 4, v244\n"
    "v_bfe_u32 v177, v0, 1, 1\n"
    "v_mul_u32_u24 v177, 4608, v177\n"
    "v_add_u32 v244, 147456, v244\n"
    "v_sub_u32 v245, v244, v177\n"
    "v_add_u32 v245, 4608, v245\n"
    "v_add_u32 v244, v244, v177\n"
    "s_lshr_b32 s51, s74, 5\n"
    "s_lshl_b32 s51, s51, 11\n"
    "s_add_u32 s51, s51, 32768\n"
    "v_min_u32 v176, 47, v0\n"
    "v_lshlrev_b32 v176, 7, v176\n"
    "v_add_u32 v177, s51, v176\n"
    "v_add_u32 v176, 6144, v176\n"
    "v_cmp_gt_u32 vcc, 16, v0\n"
    "s_nop 1\n"
    "v_cndmask_b32 v221, v176, v177, vcc\n"
    "buffer_load_dwordx2 v[240:241], v221, s[32:35], 0 offen sc1\n"
    "s_waitcnt vmcnt(0)\n"
    "v_cmp_ne_u64 vcc, s[60:61], v[240:241]\n"
    "s_nop 1\n"
    "s_cmp_lg_u64 vcc, 0\n"
    "s_cbranch_scc1 SPIN1107_%=\n"
    "BACK1109_%=:\n"
    "s_lshl_b32 s59, s44, 11\n"
    "s_add_u32 m0, s59, 135168\n"
    "s_nop 0\n"
    "buffer_load_dwordx4 v250, s[32:35], s98 offen sc1 lds\n"
    "s_add_u32 m0, s59, 135168\n"
    "s_nop 0\n"
    "buffer_load_dwordx4 v251, s[32:35], s98 offen offset:1024 sc1 lds\n"
    "buffer_load_dwordx4 v[248:251], v1, s[32:35], s99 offen offset:0 sc1\n"
    "buffer_load_dwordx4 v[252:255], v1, s[32:35], s99 offen offset:1024 sc1\n"
    "; ---- FFT ----\n"
    "v_mov_b32 v208, v242\n"
    "v_xor_b32 v209, 64, v242\n"
    "v_lshl_add_u32 v210, s44, 12, v13\n"
    "s_waitcnt vmcnt(2)\n"
    "s_barrier\n"
    "ds_read_b128 v[128:131], v210 offset:0\n"
    "ds_read_b128 v[132:135], v210 offset:1024\n"
    "ds_read_b128 a[224:227], v208 offset:0\n"
    "ds_read_b128 a[232:235], v208 offset:4096\n"
    "ds_read_b128 a[240:243], v208 offset:2048\n"
    "ds_read_b128 a[248:251], v208 offset:6144\n"
    "ds_read_b128 v[136:139], v210 offset:2048\n"
    "ds_read_b128 v[140:143], v210 offset:3072\n"
    "ds_read_b128 a[228:231], v209 offset:0\n"
    "ds_read_b128 a[236:239], v209 offset:4096\n"
    "ds_read_b128 a[244:247], v209 offset:2048\n"
    "ds_read_b128 a[252:255], v209 offset:6144\n"
    "s_waitcnt lgkmcnt(9)\n"
    "v_mfma_f32_16x16x32_bf16 v[144:147], a[224:227], v[128:131], 0\n"
    "s_waitcnt lgkmcnt(8)\n"
    "v_mfma_f32_16x16x32_bf16 v[152:155], a[232:235], v[128:131], 0\n"
    "s_waitcnt lgkmcnt(7)\n"
    "v_mfma_f32_16x16x32_bf16 v[148:151], a[240:243], v[128:131], 0\n"
    "s_waitcnt lgkmcnt(6)\n"
    "v_mfma_f32_16x16x32_bf16 v[156:159], a[248:251], v[128:131], 0\n"
    "v_mfma_f32_16x16x32_bf16 v[144:147], a[232:235], v[132:135], v[144:147]\n"
    "v_mfma_f32_16x16x32_bf16 v[160:163], a[224:227], v[132:135], 0\n"
    "v_mfma_f32_16x16x32_bf16 v[148:151], a[248:251], v[132:135], v[148:151]\n"
    "v_mfma_f32_16x16x32_bf16 v[164:167], a[240:243], v[132:135], 0\n"
    "s_waitcnt lgkmcnt(3)\n"
    "v_mfma_f32_16x16x32_bf16 v[144:147], a[228:231], v[136:139], v[144:147]\n"
    "s_waitcnt lgkmcnt(2)\n"
    "v_mfma_f32_16x16x32_bf16 v[152:155], a[236:239], v[136:139], v[152:155]\n"
    "s_waitcnt lgkmcnt(1)\n"
    "v_mfma_f32_16x16x32_bf16 v[148:151], a[244:247], v[136:139], v[148:151]\n"
    "s_waitcnt lgkmcnt(0)\n"
    "v_mfma_f32_16x16x32_bf16 v[156:159], a[252:255], v[136:139], v[156:159]\n"
    "v_mfma_f32_16x16x32_bf16 v[144:147], a[236:239], v[140:143], v[144:147]\n"
    "v_mfma_f32_16x16x32_bf16 v[160:163], a[228:231], v[140:143], v[160:163]\n"
    "v_mfma_f32_16x16x32_bf16 v[148:151], a[252:255], v[140:143], v[148:151]\n"
    "v_mfma_f32_16x16x32_bf16 v[164:167], a[244:247], v[140:143], v[164:167]\n"
    "ds_read_b128 a[224:227], v13 offset:16384\n"
    "ds_read_b128 a[228:231], v13 offset:17408\n"
    "ds_read_b128 a[232:235], v13 offset:18432\n"
    "ds_read_b128 a[236:239], v13 offset:19456\n"
    "s_nop 1\n"
    "v_pk_add_f32 v[152:153], v[152:153], v[160:161] neg_lo:[0,1] neg_hi:[0,1]\n"
    "v_pk_add_f32 v[154:155], v[154:155], v[162:163] neg_lo:[0,1] neg_hi:[0,1]\n"
    "v_pk_add_f32 v[156:157], v[156:157], v[164:165] neg_lo:[0,1] neg_hi:[0,1]\n"
    "v_pk_add_f32 v[158:159], v[158:159], v[166:167] neg_lo:[0,1] neg_hi:[0,1]\n"
    "v_pk_mul_f32 v[128:129], v[144:145], v[224:225]\n"
    "v_pk_mul_f32 v[136:137], v[152:153], v[224:225]\n"
    "v_pk_fma_f32 v[128:129], v[152:153], v[232:233], v[128:129]\n"
    "v_pk_fma_f32 v[136:137], v[144:145], v[232:233], v[136:137] neg_lo:[1,0,0] neg_hi:[1,0,0]\n"
    "v_pk_mul_f32 v[130:131], v[146:147], v[226:227]\n"
    "v_pk_mul_f32 v[138:139], v[154:155], v[226:227]\n"
    "v_pk_fma_f32 v[130:131], v[154:155], v[234:235], v[130:131]\n"
    "v_pk_fma_f32 v[138:139], v[146:147], v[234:235], v[138:139] neg_lo:[1,0,0] neg_hi:[1,0,0]\n"
    "v_pk_mul_f32 v[132:133], v[148:149], v[228:229]\n"
    "v_pk_mul_f32 v[140:141], v[156:157], v[228:229]\n"
    "v_pk_fma_f32 v[132:133], v[156:157], v[236:237], v[132:133]\n"
    "v_pk_fma_f32 v[140:141], v[148:149], v[236:237], v[140:141] neg_lo:[1,0,0] neg_hi:[1,0,0]\n"
    "v_pk_mul_f32 v[134:135], v[150:151], v[230:231]\n"
    "v_pk_mul_f32 v[142:143], v[158:159], v[230:231]\n"
    "v_pk_fma_f32 v[134:135], v[158:159], v[238:239], v[134:135]\n"
    "v_pk_fma_f32 v[142:143], v[150:151], v[238:239], v[142:143] neg_lo:[1,0,0] neg_hi:[1,0,0]\n"
    "v_cvt_pk_bf16_f32 v168, v128, v129\n"
    "v_cvt_pk_bf16_f32 v169, v130, v131\n"
    "v_cvt_pk_bf16_f32 v170, v132, v133\n"
    "v_cvt_pk_bf16_f32 v171, v134, v135\n"
    "v_cvt_pk_bf16_f32 v172, v136, v137\n"
    "v_cvt_pk_bf16_f32 v173, v138, v139\n"
    "v_cvt_pk_bf16_f32 v174, v140, v141\n"
    "v_cvt_pk_bf16_f32 v175, v142, v143\n"
    "v_cvt_pk_bf16_f32 v176, -v128, -v129\n"
    "v_cvt_pk_bf16_f32 v177, -v130, -v131\n"
    "v_cvt_pk_bf16_f32 v178, -v132, -v133\n"
    "v_cvt_pk_bf16_f32 v179, -v134, -v135\n"
    "s_waitcnt lgkmcnt(3)\n"
    "v_mfma_f32_16x16x32_bf16 v[144:147], a[224:227], v[168:171], 0\n"
    "s_waitcnt vmcnt(1)\n"
    "v_lshlrev_b32 v180, 16, v248\n"
    "v_and_b32 v188, s77, v248\n"
    "v_mfma_f32_16x16x32_bf16 v[152:155], a[224:227], v[172:175], 0\n"
    "v_lshlrev_b32 v181, 16, v249\n"
    "v_and_b32 v189, s77, v249\n"
    "s_waitcnt lgkmcnt(1)\n"
    "v_mfma_f32_16x16x32_bf16 v[148:151], a[232:235], v[168:171], 0\n"
    "v_lshlrev_b32 v182, 16, v250\n"
    "v_and_b32 v190, s77, v250\n"
    "v_mfma_f32_16x16x32_bf16 v[156:159], a[232:235], v[172:175], 0\n"
    "v_lshlrev_b32 v183, 16, v251\n"
    "v_and_b32 v191, s77, v251\n"
    "v_mfma_f32_16x16x32_bf16 v[144:147], a[228:231], v[172:175], v[144:147]\n"
    "s_waitcnt vmcnt(0)\n"
    "v_lshlrev_b32 v184, 16, v252\n"
    "v_and_b32 v192, s77, v252\n"
    "v_mfma_f32_16x16x32_bf16 v[152:155], a[228:231], v[176:179], v[152:155]\n"
    "v_lshlrev_b32 v185, 16, v253\n"
    "v_and_b32 v193, s77, v253\n"
    "s_waitcnt lgkmcnt(0)\n"
    "v_mfma_f32_16x16x32_bf16 v[148:151], a[236:239], v[172:175], v[148:151]\n"
    "v_lshlrev_b32 v186, 16, v254\n"
    "v_and_b32 v194, s77, v254\n"
    "v_mfma_f32_16x16x32_bf16 v[156:159], a[236:239], v[176:179], v[156:159]\n"
    "v_lshlrev_b32 v187, 16, v255\n"
    "v_and_b32 v195, s77, v255\n"
    "v_pk_mul_f32 v[128:129], v[144:145], v[180:181]\n"
    "v_pk_mul_f32 v[136:137], v[152:153], v[180:181]\n"
    "v_pk_fma_f32 v[128:129], v[152:153], v[188:189], v[128:129] neg_lo:[1,0,0] neg_hi:[1,0,0]\n"
    "v_pk_fma_f32 v[136:137], v[144:145], v[188:189], v[136:137]\n"
    "v_pk_mul_f32 v[130:131], v[146:147], v[182:183]\n"
    "v_pk_mul_f32 v[138:139], v[154:155], v[182:183]\n"
    "v_pk_fma_f32 v[130:131], v[154:155], v[190:191], v[130:131] neg_lo:[1,0,0] neg_hi:[1,0,0]\n"
    "v_pk_fma_f32 v[138:139], v[146:147], v[190:191], v[138:139]\n"
    "v_pk_mul_f32 v[132:133], v[148:149], v[184:185]\n"
    "v_pk_mul_f32 v[140:141], v[156:157], v[184:185]\n"
    "v_pk_fma_f32 v[132:133], v[156:157], v[192:193], v[132:133] neg_lo:[1,0,0] neg_hi:[1,0,0]\n"
    "v_pk_fma_f32 v[140:141], v[148:149], v[192:193], v[140:141]\n"
    "v_pk_mul_f32 v[134:135], v[150:151], v[186:187]\n"
    "v_pk_mul_f32 v[142:143], v[158:159], v[186:187]\n"
    "v_pk_fma_f32 v[134:135], v[158:159], v[194:195], v[134:135] neg_lo:[1,0,0] neg_hi:[1,0,0]\n"
    "v_pk_fma_f32 v[142:143], v[150:151], v[194:195], v[142:143]\n"
    "v_cvt_pk_bf16_f32 v168, v128, v129\n"
    "v_cvt_pk_bf16_f32 v169, v130, v131\n"
    "v_cvt_pk_bf16_f32 v170, v132, v133\n"
    "v_cvt_pk_bf16_f32 v171, v134, v135\n"
    "v_cvt_pk_bf16_f32 v172, v136, v137\n"
    "v_cvt_pk_bf16_f32 v173, v138, v139\n"
    "v_cvt_pk_bf16_f32 v174, v140, v141\n"
    "v_cvt_pk_bf16_f32 v175, v142, v143\n"
    "v_cvt_pk_bf16_f32 v176, -v136, -v137\n"
    "v_cvt_pk_bf16_f32 v177, -v138, -v139\n"
    "v_cvt_pk_bf16_f32 v178, -v140, -v141\n"
    "v_cvt_pk_bf16_f32 v179, -v142, -v143\n"
    "v_mfma_f32_16x16x32_bf16 v[144:147], a[224:227], v[168:171], 0\n"
    "v_mfma_f32_16x16x32_bf16 v[152:155], a[224:227], v[172:175], 0\n"
    "v_mfma_f32_16x16x32_bf16 v[148:151], a[232:235], v[168:171], 0\n"
    "v_mfma_f32_16x16x32_bf16 v[156:159], a[232:235], v[172:175], 0\n"
    "v_mfma_f32_16x16x32_bf16 v[144:147], a[228:231], v[176:179], v[144:147]\n"
    "v_mfma_f32_16x16x32_bf16 v[152:155], a[228:231], v[168:171], v[152:155]\n"
    "v_mfma_f32_16x16x32_bf16 v[148:151], a[236:239], v[176:179], v[148:151]\n"
    "v_mfma_f32_16x16x32_bf16 v[156:159], a[236:239], v[168:171], v[156:159]\n"
    "ds_read_b128 v[180:183], v13 offset:9216\n"
    "ds_read_b128 v[184:187], v13 offset:10240\n"
    "ds_read_b128 v[188:191], v13 offset:11264\n"
    "ds_read_b128 v[192:195], v13 offset:12288\n"
    "ds_read_b128 v[196:199], v13 offset:13312\n"
    "ds_read_b128 v[200:203], v13 offset:14336\n"
    "ds_read_b128 v[204:207], v13 offset:15360\n"
    "ds_read_b128 a[240:243], v13 offset:4096\n"
    "ds_read_b128 a[244:247], v13 offset:5120\n"
    "ds_read_b128 a[248:251], v13 offset:6144\n"
    "ds_read_b128 a[252:255], v13 offset:7168\n"
    "v_pk_mul_f32 v[128:129], v[144:145], v[224:225]\n"
    "v_pk_mul_f32 v[136:137], v[152:153], v[224:225]\n"
    "v_pk_fma_f32 v[128:129], v[152:153], v[232:233], v[128:129] neg_lo:[1,0,0] neg_hi:[1,0,0]\n"
    "v_pk_fma_f32 v[136:137], v[144:145], v[232:233], v[136:137]\n"
    "v_pk_mul_f32 v[130:131], v[146:147], v[226:227]\n"
    "v_pk_mul_f32 v[138:139], v[154:155], v[226:227]\n"
    "v_pk_fma_f32 v[130:131], v[154:155], v[234:235], v[130:131] neg_lo:[1,0,0] neg_hi:[1,0,0]\n"
    "v_pk_fma_f32 v[138:139], v[146:147], v[234:235], v[138:139]\n"
    "v_pk_mul_f32 v[132:133], v[148:149], v[228:229]\n"
    "v_pk_mul_f32 v[140:141], v[156:157], v[228:229]\n"
    "v_pk_fma_f32 v[132:133], v[156:157], v[236:237], v[132:133] neg_lo:[1,0,0] neg_hi:[1,0,0]\n"
    "v_pk_fma_f32 v[140:141], v[148:149], v[236:237], v[140:141]\n"
    "v_pk_mul_f32 v[134:135], v[150:151], v[230:231]\n"
    "v_pk_mul_f32 v[142:143], v[158:159], v[230:231]\n"
    "v_pk_fma_f32 v[134:135], v[158:159], v[238:239], v[134:135] neg_lo:[1,0,0] neg_hi:[1,0,0]\n"
    "v_pk_fma_f32 v[142:143], v[150:151], v[238:239], v[142:143]\n"
    "v_cvt_pk_bf16_f32 v144, v128, v129\n"
    "v_cvt_pk_bf16_f32 v145, v130, v131\n"
    "v_cvt_pk_bf16_f32 v146, v132, v133\n"
    "v_cvt_pk_bf16_f32 v147, v134, v135\n"
    "v_cvt_pk_bf16_f32 v148, v136, v137\n"
    "v_cvt_pk_bf16_f32 v149, v138, v139\n"
    "v_cvt_pk_bf16_f32 v150, v140, v141\n"
    "v_cvt_pk_bf16_f32 v151, v142, v143\n"
    "ds_write_b64 v243, v[144:145] offset:0\n"
    "ds_write_b64 v243, v[146:147] offset:32\n"
    "ds_write_b64 v243, v[148:149] offset:4608\n"
    "ds_write_b64 v243, v[150:151] offset:4640\n"
    "ds_read_b128 a[224:227], v13 offset:0\n"
    "ds_read_b128 a[228:231], v13 offset:1024\n"
    "ds_read_b128 a[232:235], v13 offset:2048\n"
    "ds_read_b128 a[236:239], v13 offset:3072\n"
    "ds_read_b128 v[176:179], v13 offset:8192\n"
    "s_waitcnt lgkmcnt(0)\n"
    "s_barrier\n"
    "ds_read_b64_tr_b16 v[128:129], v244 offset:0\n"
    "ds_read_b64_tr_b16 v[130:131], v244 offset:576\n"
    "ds_read_b64_tr_b16 v[132:133], v244 offset:2304\n"
    "ds_read_b64_tr_b16 v[134:135], v244 offset:2880\n"
    "ds_read_b64_tr_b16 v[136:137], v245 offset:0\n"
    "ds_read_b64_tr_b16 v[138:139], v245 offset:576\n"
    "ds_read_b64_tr_b16 v[140:141], v245 offset:2304\n"
    "ds_read_b64_tr_b16 v[142:143], v245 offset:2880\n"
    "v_and_b32 v208, 15, v0\n"
    "v_mul_u32_u24 v208, 144, v208\n"
    "v_lshrrev_b32 v209, 4, v0\n"
    "v_lshl_add_u32 v208, v209, 4, v208\n"
    "s_mul_i32 s59, s44, 2304\n"
    "s_add_u32 s59, s59, 104448\n"
    "v_add_u32 v208, s59, v208\n"
    "v_lshrrev_b32 v209, 3, v0\n"
    "v_mul_u32_u24 v209, 144, v209\n"
    "v_and_b32 v210, 7, v0\n"
    "s_waitcnt lgkmcnt(6)\n"
    "v_mfma_f32_16x16x32_bf16 v[144:147], a[224:227], v[128:131], 0\n"
    "s_waitcnt lgkmcnt(3)\n"
    "v_xor_b32 v136, v246, v136\n"
    "v_mfma_f32_16x16x32_bf16 v[148:151], a[240:243], v[128:131], 0\n"
    "v_xor_b32 v137, v246, v137\n"
    "v_mfma_f32_16x16x32_bf16 v[152:155], v[176:179], v[128:131], 0\n"
    "s_waitcnt lgkmcnt(2)\n"
    "v_xor_b32 v138, v246, v138\n"
    "v_mfma_f32_16x16x32_bf16 v[156:159], v[192:195], v[128:131], 0\n"
    "v_xor_b32 v139, v246, v139\n"
    "v_mfma_f32_16x16x32_bf16 v[144:147], a[232:235], v[132:135], v[144:147]\n"
    "s_waitcnt lgkmcnt(1)\n"
    "v_xor_b32 v140, v246, v140\n"
    "v_mfma_f32_16x16x32_bf16 v[148:151], a[248:251], v[132:135], v[148:151]\n"
    "v_xor_b32 v141, v246, v141\n"
    "v_mfma_f32_16x16x32_bf16 v[152:155], v[184:187], v[132:135], v[152:155]\n"
    "s_waitcnt lgkmcnt(0)\n"
    "v_xor_b32 v142, v246, v142\n"
    "v_mfma_f32_16x16x32_bf16 v[156:159], v[200:203], v[132:135], v[156:159]\n"
    "v_xor_b32 v143, v246, v143\n"
    "v_mfma_f32_16x16x32_bf16 v[144:147], a[228:231], v[136:139], v[144:147]\n"
    "v_lshl_add_u32 v209, v210, 4, v209\n"
    "v_mfma_f32_16x16x32_bf16 v[148:151], a[244:247], v[136:139], v[148:151]\n"
    "v_add_u32 v209, s59, v209\n"
    "v_mfma_f32_16x16x32_bf16 v[152:155], v[180:183], v[136:139], v[152:155]\n"
    "s_or_b32 s59, s75, 128\n"
    "v_mfma_f32_16x16x32_bf16 v[156:159], v[196:199], v[136:139], v[156:159]\n"
    "s_add_u32 s59, s59, s79\n"
    "v_mfma_f32_16x16x32_bf16 v[144:147], a[236:239], v[140:143], v[144:147]\n"
    "v_mfma_f32_16x16x32_bf16 v[148:151], a[252:255], v[140:143], v[148:151]\n"
    "v_mfma_f32_16x16x32_bf16 v[152:155], v[188:191], v[140:143], v[152:155]\n"
    "v_mfma_f32_16x16x32_bf16 v[156:159], v[204:207], v[140:143], v[156:159]\n"
    "s_nop 4\n"
    "v_cvt_pk_bf16_f32 v160, v144, v145\n"
    "v_cvt_pk_bf16_f32 v161, v146, v147\n"
    "v_cvt_pk_bf16_f32 v162, v148, v149\n"
    "v_cvt_pk_bf16_f32 v163, v150, v151\n"
    "ds_write_b128 v208, v[160:163] offset:0\n"
    "v_cvt_pk_bf16_f32 v164, v152, v153\n"
    "v_cvt_pk_bf16_f32 v165, v154, v155\n"
    "v_cvt_pk_bf16_f32 v166, v156, v157\n"
    "v_cvt_pk_bf16_f32 v167, v158, v159\n"
    "ds_write_b128 v208, v[164:167] offset:64\n"
    "ds_read_b128 v[128:131], v209 offset:0\n"
    "ds_read_b128 v[132:135], v209 offset:1152\n"
    "s_waitcnt lgkmcnt(1)\n"
    "buffer_store_dwordx4 v[128:131], v247, s[32:35], s79 offen sc1\n"
    "s_waitcnt lgkmcnt(0)\n"
    "buffer_store_dwordx4 v[132:135], v247, s[32:35], s59 offen sc1\n"
    "s_mov_b32 s78, 132864\n"
    "s_waitcnt vmcnt(0) lgkmcnt(0)\n"
    "s_barrier\n"
    "s_cmp_lg_u32 s44, 0\n"
    "s_cbranch_scc1 NP1110_%=\n"
    "s_mov_b64 exec, 1\n"
    "buffer_store_dwordx2 v[222:223], off, s[32:35], s78 sc1\n"
    "s_mov_b64 exec, -1\n"
    "NP1110_%=:\n"
    "s_mov_b32 s78, 132872\n"
    "s_cmp_lg_u32 s44, 0\n"
    "s_cbranch_scc1 NP1111_%=\n"
    "s_mov_b64 exec, 1\n"
    "buffer_store_dwordx2 v[222:223], off, s[32:35], s78 sc1\n"
    "s_mov_b64 exec, -1\n"
    "NP1111_%=:\n"
    "AUXDONE1106_%=:\n"
    "s_nop 7\n"
    "s_nop 3\n"
    "s_endpgm\n"
    "SPIN1107_%=:\n"
    "s_mov_b32 s70, 0\n"
    "LOOP1108_%=:\n"
    "s_sleep 4\n"
    "buffer_load_dwordx2 v[240:241], v221, s[32:35], 0 offen sc1\n"
    "s_waitcnt vmcnt(0)\n"
    "v_cmp_ne_u64 vcc, s[60:61], v[240:241]\n"
    "s_nop 1\n"
    "s_cmp_lg_u64 vcc, 0\n"
    "s_cbranch_scc0 BACK1109_%=\n"
    "s_add_u32 s70, s70, 1\n"
    "s_cmp_lt_u32 s70, 20000\n"
    "s_cbranch_scc1 LOOP1108_%=\n"
    "s_endpgm\n"
    "KEND_%=:\n"
    : "+{v0}"(tid)
    : "{s[0:1]}"(__builtin_amdgcn_kernarg_segment_ptr()), "{s2}"(blockIdx.x), "{s[20:21]}"(tab), "{s23}"((unsigned)(size_t)lds)
    : "memory", "vcc", "scc", "v1", "v2", "v3", "v4", "v5", "v6", "v7", "v8", "v9", "v10", "v11", "v12", "v13", "v14", "v15", "v16",
      "v17", "v18", "v19", "v20", "v21", "v22", "v23", "v24", "v25", "v26", "v27", "v28", "v29", "v30", "v31", "v32",
      "v33", "v34", "v35", "v36", "v37", "v38", "v39", "v40", "v41", "v42", "v43", "v44", "v45", "v46", "v47", "v48",
      "v49", "v50", "v51", "v52", "v53", "v54", "v55", "v56", "v57", "v58", "v59", "v60", "v61", "v62", "v63", "v64",
      "v65", "v66", "v67", "v68", "v69", "v70", "v71", "v72", "v73", "v74", "v75", "v76", "v77", "v78", "v79", "v80",
      "v81", "v82", "v83", "v84", "v85", "v86", "v87", "v88", "v89", "v90", "v91", "v92", "v93", "v94", "v95", "v96",
      "v97", "v98", "v99", "v100", "v101", "v102", "v103", "v104", "v105", "v106", "v107", "v108", "v109", "v110", "v111", "v112",
      "v113", "v114", "v115", "v116", "v117", "v118", "v119", "v120", "v121", "v122", "v123", "v124", "v125", "v126", "v127", "v128",
      "v129", "v130", "v131", "v132", "v133", "v134", "v135", "v136", "v137", "v138", "v139", "v140", "v141", "v142", "v143", "v144",
      "v145", "v146", "v147", "v148", "v149", "v150", "v151", "v152", "v153", "v154", "v155", "v156", "v157", "v158", "v159", "v160",
      "v161", "v162", "v163", "v164", "v165", "v166", "v167", "v168", "v169", "v170", "v171", "v172", "v173", "v174", "v175", "v176",
      "v177", "v178", "v179", "v180", "v181", "v182", "v183", "v184", "v185", "v186", "v187", "v188", "v189", "v190", "v191", "v192",
      "v193", "v194", "v195", "v196", "v197", "v198", "v199", "v200", "v201", "v202", "v203", "v204", "v205", "v206", "v207", "v208",
      "v209", "v210", "v211", "v212", "v213", "v214", "v215", "v216", "v217", "v218", "v219", "v220", "v221", "v222", "v223", "v224",
      "v225", "v226", "v227", "v228", "v229", "v230", "v231", "v232", "v233", "v234", "v235", "v236", "v237", "v238", "v239", "v240",
      "v241", "v242", "v243", "v244", "v245", "v246", "v247", "v248", "v249", "v250", "v251", "v252", "v253", "v254", "v255", "a0",
      "a1", "a2", "a3", "a4", "a5", "a6", "a7", "a8", "a9", "a10", "a11", "a12", "a13", "a14", "a15", "a16",
      "a17", "a18", "a19", "a20", "a21", "a22", "a23", "a24", "a25", "a26", "a27", "a28", "a29", "a30", "a31", "a32",
      "a33", "a34", "a35", "a36", "a37", "a38", "a39", "a40", "a41", "a42", "a43", "a44", "a45", "a46", "a47", "a48",
      "a49", "a50", "a51", "a52", "a53", "a54", "a55", "a56", "a57", "a58", "a59", "a60", "a61", "a62", "a63", "a64",
      "a65", "a66", "a67", "a68", "a69", "a70", "a71", "a72", "a73", "a74", "a75", "a76", "a77", "a78", "a79", "a80",
      "a81", "a82", "a83", "a84", "a85", "a86", "a87", "a88", "a89", "a90", "a91", "a92", "a93", "a94", "a95", "a96",
      "a97", "a98", "a99", "a100", "a101", "a102", "a103", "a104", "a105", "a106", "a107", "a108", "a109", "a110", "a111", "a112",
      "a113", "a114", "a115", "a116", "a117", "a118", "a119", "a120", "a121", "a122", "a123", "a124", "a125", "a126", "a127", "a128",
      "a129", "a130", "a131", "a132", "a133", "a134", "a135", "a136", "a137", "a138", "a139", "a140", "a141", "a142", "a143", "a144",
      "a145", "a146", "a147", "a148", "a149", "a150", "a151", "a152", "a153", "a154", "a155", "a156", "a157", "a158", "a159", "a160",
      "a161", "a162", "a163", "a164", "a165", "a166", "a167", "a168", "a169", "a170", "a171", "a172", "a173", "a174", "a175", "a176",
      "a177", "a178", "a179", "a180", "a181", "a182", "a183", "a184", "a185", "a186", "a187", "a188", "a189", "a190", "a191", "a192",
      "a193", "a194", "a195", "a196", "a197", "a198", "a199", "a200", "a201", "a202", "a203", "a204", "a205", "a206", "a207", "a208",
      "a209", "a210", "a211", "a212", "a213", "a214", "a215", "a216", "a217", "a218", "a219", "a220", "a221", "a222", "a223", "a224",
      "a225", "a226", "a227", "a228", "a229", "a230", "a231", "a232", "a233", "a234", "a235", "a236", "a237", "a238", "a239", "a240",
      "a241", "a242", "a243", "a244", "a245", "a246", "a247", "a248", "a249", "a250", "a251", "a252", "a253", "a254", "a255", "s3",
      "s4", "s5", "s6", "s7", "s8", "s9", "s10", "s11", "s12", "s13", "s14", "s15", "s16", "s17", "s18", "s19",
      "s22", "s24", "s25", "s26", "s27", "s28", "s29", "s30", "s31", "s33", "s34", "s35", "s36", "s37", "s38", "s39",
      "s40", "s41", "s42", "s43", "s44", "s45", "s46", "s47", "s48", "s49", "s50", "s51", "s52", "s53", "s54", "s55",
      "s56", "s57", "s58", "s59", "s60", "s61", "s62", "s63", "s64", "s65", "s66", "s67", "s68", "s69", "s70", "s71",
      "s72", "s73", "s74", "s75", "s76", "s77", "s78", "s79", "s80", "s81", "s82", "s83", "s84", "s85", "s86", "s87",
      "s88", "s89", "s90", "s91", "s92", "s93", "s94", "s95", "s96", "s97", "s98", "s99");
#endif
}

extern "C" void kernel_launch(void* const* d_in, const int* in_sizes, int n_in, void* d_out, int out_size, void* d_ws, size_t ws_size,
                              hipStream_t stream) {
  gfnet_swiglu_kernel<<<256, 256, 0, stream>>>(d_in[0], d_in[1], d_in[2], d_in[4], d_in[5], d_in[6], d_out, d_ws);
}
```
